```python
import torch

def _softmax(x, axis=-1):
    return torch.softmax(x, dim=axis)
def _gelu(x):
    return torch.nn.functional.gelu(x, approximate="tanh")
def _silu(x):
    return torch.nn.functional.silu(x)
def _sigmoid(x):
    return torch.sigmoid(x)
def _layernorm(x, axis=-1, eps=1e-5):
    m = x.mean(dim=axis, keepdim=True)
    v = x.var(dim=axis, keepdim=True, unbiased=False)
    return (x - m) / torch.sqrt(v + eps)
def _scatter_add(tgt, idx, src, axis=0):
    return tgt.clone().index_add_(axis, idx, src)
def _segment_sum(src, seg, n):
    out = torch.zeros((n,) + src.shape[1:], dtype=src.dtype)
    return out.index_add_(0, seg, src)
def _pad(x, pw):
    flat = tuple(v for pair in reversed(pw) for v in pair)
    return torch.nn.functional.pad(x, flat)

kernel_name = 'recurrent_scan__ssm_conv_s4__mixer_s_mlp__prompt'

def setup_inputs():

    _w = (lambda a: (lambda t: t.to(torch.bfloat16) if t.is_floating_point() else t)(torch.as_tensor(a)))
    def _A_setup():
        B, L, D, N = 1, 2048, 448, 256
        A_re = -(lambda _x: torch.exp(torch.as_tensor(_x)))((lambda *s: torch.randn(*s, dtype=torch.bfloat16) * 3.2)(D, N))
        A_im = torch.pi * (lambda *s: torch.randn(*s, dtype=torch.bfloat16) * 0.05)(D, N)
        B_re = (lambda *s: torch.randn(*s, dtype=torch.bfloat16) * 0.1)(D, N)
        B_im = (lambda *s: torch.randn(*s, dtype=torch.bfloat16) * 0.1)(D, N)
        C_re = (lambda *s: torch.randn(*s, dtype=torch.bfloat16) * 0.1)(D, N)
        C_im = (lambda *s: torch.randn(*s, dtype=torch.bfloat16) * 0.1)(D, N)
        log_dt = (lambda *s: torch.randn(*s, dtype=torch.bfloat16) * 0.05)(D) * 0.5 - 4.0
        Dskip = (lambda *s: torch.randn(*s, dtype=torch.bfloat16) * 0.05)(D)
        return {
            "u": _w((lambda *s: torch.randn(*s, dtype=torch.bfloat16) * 0.05)(B, L, D)),
            "A_re": _w(A_re),
            "A_im": _w(A_im),
            "B_re": _w(B_re),
            "B_im": _w(B_im),
            "C_re": _w(C_re),
            "C_im": _w(C_im),
            "log_dt": _w(log_dt),
            "Dskip": _w(Dskip),
        }
    _A_in = _A_setup()

    def _B_setup():
        B, S, D, F = 1, 2048, 448, 1792
        return {
            "x":  _w((lambda *s: torch.randn(*s, dtype=torch.bfloat16) * 0.05)(B, S, D)),
            "w1": _w((lambda *s: torch.randn(*s, dtype=torch.bfloat16) * 3.2)(D, D) * (D ** -0.5)),
            "w2": _w((lambda *s: torch.randn(*s, dtype=torch.bfloat16) * 3.2)(D, D) * (D ** -0.5)),
            "w3": _w((lambda *s: torch.randn(*s, dtype=torch.bfloat16) * 3.2)(D, F) * (D ** -0.5)),
            "w4": _w((lambda *s: torch.randn(*s, dtype=torch.bfloat16) * 3.2)(F, D) * (F ** -0.5)),
        }
    _B_in = _B_setup()

    return {
        "A_u": _A_in["u"],
        "A_A_re": _A_in["A_re"],
        "A_A_im": _A_in["A_im"],
        "A_B_re": _A_in["B_re"],
        "A_B_im": _A_in["B_im"],
        "A_C_re": _A_in["C_re"],
        "A_C_im": _A_in["C_im"],
        "A_log_dt": _A_in["log_dt"],
        "A_Dskip": _A_in["Dskip"],
        "B_x": _B_in["x"],
        "B_w1": _B_in["w1"],
        "B_w2": _B_in["w2"],
        "B_w3": _B_in["w3"],
        "B_w4": _B_in["w4"],
    }

def reference(A_u, A_A_re, A_A_im, A_B_re, A_B_im, A_C_re, A_C_im, A_log_dt, A_Dskip, B_x, B_w1, B_w2, B_w3, B_w4):
    def _A_ref(u, A_re, A_im, B_re, B_im, C_re, C_im, log_dt, Dskip):
        Bsz, L, D = u.shape
        dt = torch.exp(log_dt)[:, None]
        A = A_re + 1j * A_im
        Bc = B_re + 1j * B_im
        Cc = C_re + 1j * C_im
        logdA = dt * A
        dB = dt * Bc
        l = (lambda x, dt: x.to(dt))(torch.arange(L), A_re.dtype)
        P = torch.exp(logdA[:, :, None] * l[None, None, :])
        K = torch.einsum("dn,dnl,dn->dl", Cc, P, dB).real
        L2 = 2 * L
        Kp = _pad(K, ((0, 0), (0, L)))
        up = (lambda x, *d: x.permute(*d))(u, 0, 2, 1)
        up = _pad(up, ((0, 0), (0, 0), (0, L)))
        Kf = (lambda x, axes=(-2,-1): torch.fft.rfft2(x.float(), dim=axes))(Kp[:, None, :], axes=(-2, -1))
        Uf = (lambda x, axes=(-2,-1): torch.fft.rfft2(x.float(), dim=axes))(up[:, :, None, :], axes=(-2, -1))
        Yf = Uf * Kf[None, :, :, :]
        y = (lambda x, s, axes=(-2,-1): torch.fft.irfft2(x, s=s, dim=axes).to(torch.bfloat16))(Yf, (1, L2), axes=(-2, -1))[:, :, 0, :L]
        y = (lambda x, *d: x.permute(*d))(y, 0, 2, 1) + u * Dskip[None, None, :]
        return y

    def _B_ref(x, w1, w2, w3, w4):
        B, S, D = x.shape
        h = _gelu(_layernorm(x, axis=-1) @ w1)
        c = D // 4
        g0 = _pad(h[:, :, 0*c:1*c], ((0, 0), (1, 0), (0, 0)))[:, :S, :]
        g1 = _pad(h[:, :, 1*c:2*c], ((0, 0), (0, 1), (0, 0)))[:, 1:, :]
        g2 = _pad(h[:, :, 2*c:3*c], ((0, 0), (2, 0), (0, 0)))[:, :S, :]
        g3 = _pad(h[:, :, 3*c:   ], ((0, 0), (0, 2), (0, 0)))[:, 2:, :]
        hs = torch.concatenate([g0, g1, g2, g3], axis=-1) @ w2
        x1 = x + hs
        h2 = _gelu(_layernorm(x1, axis=-1) @ w3) @ w4
        return x1 + h2

    x = A_u
    xn = _layernorm(x, axis=-1)
    s = _A_ref(xn, A_A_re, A_A_im, A_B_re, A_B_im, A_C_re, A_C_im, A_log_dt, A_Dskip)
    g = _silu(_B_ref(xn, B_w1, B_w2, B_w3, B_w4))
    return x + s * g
```

```cpp
#include <hip/hip_runtime.h>
#include <hip/hip_bf16.h>
#include <stdint.h>
typedef unsigned short u16;
constexpr int L = 2048, D = 448, NS = 256, F = 1792;
__device__ __forceinline__ float b2f(u16 b) { return __builtin_bit_cast(float, (unsigned)b << 16); }
__device__ __forceinline__ u16 f2b(float f) { __hip_bfloat16 h = __float2bfloat16(f); return __builtin_bit_cast(u16, h); }
__device__ __forceinline__ float wsum(float v) { for (int o = 32; o; o >>= 1) v += __shfl_xor(v, o); return v; }
__global__ void k_ln(const u16* x, u16* y, int rows) {
  int row = blockIdx.x * 4 + (threadIdx.x >> 6), lane = threadIdx.x & 63;
  float v[7]; float s = 0;
  for (int i = 0; i < 7; i++) { v[i] = b2f(x[row * D + lane + 64 * i]); s += v[i]; }
  float m = wsum(s) / D; float q = 0;
  for (int i = 0; i < 7; i++) { v[i] -= m; q += v[i] * v[i]; }
  float r = rsqrtf(wsum(q) / D + 1e-5f);
  for (int i = 0; i < 7; i++) y[row * D + lane + 64 * i] = f2b(v[i] * r);
}
__global__ void k_kgen(const u16* Are, const u16* Aim, const u16* Bre, const u16* Bim, const u16* Cre, const u16* Cim,
                       const u16* ldt, float* K) {
  __shared__ float sa[NS], sb[NS], sc[NS], sd[NS], kv[648];
  int d = blockIdx.x, n = threadIdx.x;
  float dt = b2f(f2b(expf(b2f(ldt[d]))));
  float br = dt * b2f(Bre[d * NS + n]), bi = dt * b2f(Bim[d * NS + n]);
  float cr = b2f(Cre[d * NS + n]), ci = b2f(Cim[d * NS + n]);
  sa[n] = dt * b2f(Are[d * NS + n]); sb[n] = dt * b2f(Aim[d * NS + n]);
  sc[n] = cr * br - ci * bi; sd[n] = cr * bi + ci * br;
  __syncthreads();
  for (int i = n; i < 641; i += 256) {
    int l = i < 256 ? i : i < 384 ? 256 + 2 * (i - 256) : i < 512 ? 512 + 4 * (i - 384) : 1024 + 8 * (i - 512);
    float acc = 0;
    for (int m = 0; m < NS; m++) {
      float e = __expf(sa[m] * l), sn, cs; __sincosf(sb[m] * l, &sn, &cs);
      acc += e * (sc[m] * cs - sd[m] * sn);
    }
    kv[i] = acc;
  }
  __syncthreads();
  for (int l = n; l < L; l += 256) {
    int lr = (int)b2f(f2b((float)l));
    int i = lr < 256 ? lr : lr < 512 ? 256 + (lr - 256) / 2 : lr < 1024 ? 384 + (lr - 512) / 4 : 512 + (lr - 1024) / 8;
    K[d * L + l] = kv[i];
  }
}
__global__ void k_conv(const u16* xn, const float* K, const u16* Dk, u16* s) {
  __shared__ float su[L], sk[L];
  int d = blockIdx.x;
  for (int l = threadIdx.x; l < L; l += 256) { su[l] = b2f(xn[l * D + d]); sk[l] = K[d * L + l]; }
  __syncthreads();
  float dk = b2f(Dk[d]);
  for (int t = threadIdx.x; t < L; t += 256) {
    float acc = 0;
    for (int m = 0; m <= t; m++) acc += sk[m] * su[t - m];
    s[t * D + d] = f2b(b2f(f2b(acc)) + b2f(f2b(su[t] * dk)));
  }
}
typedef short bf16x8 __attribute__((ext_vector_type(8)));
typedef float f32x4 __attribute__((ext_vector_type(4)));
__device__ __forceinline__ float gelu(float x) { float z = 0.7978845608f * (x + 0.044715f * x * x * x); return x / (1.f + __expf(-2.f * z)); }
template <int MODE>
__global__ void k_gemm(const u16* A, const u16* W, u16* C, int K, int N, const u16* res, const u16* x, const u16* s) {
  int wave = threadIdx.x >> 6, lane = threadIdx.x & 63;
  int row0 = blockIdx.y * 64 + wave * 16, col0 = blockIdx.x * 16;
  f32x4 acc = {0, 0, 0, 0};
  for (int k0 = 0; k0 < K; k0 += 32) {
    int k = k0 + 8 * (lane >> 4);
    int r = row0 + (lane & 15);
    bf16x8 a, b;
    if (MODE == 1) { int g = k / 112; r -= (g == 0 ? 1 : g == 1 ? -1 : g == 2 ? 2 : -2); }
    if (r >= 0 && r < L) a = *(const bf16x8*)(A + r * K + k); else a = bf16x8{0,0,0,0,0,0,0,0};
    for (int j = 0; j < 8; j++) b[j] = W[(k + j) * N + col0 + (lane & 15)];
    acc = __builtin_amdgcn_mfma_f32_16x16x32_bf16(a, b, acc, 0, 0, 0);
  }
  for (int j = 0; j < 4; j++) {
    int r = row0 + 4 * (lane >> 4) + j, c = col0 + (lane & 15);
    float v = b2f(f2b(acc[j]));
    if (MODE == 0 || MODE == 2) v = gelu(v);
    if (MODE == 1) v += b2f(res[r * N + c]);
    if (MODE == 3) { v = b2f(f2b(v + b2f(res[r * N + c]))); float g = v / (1.f + __expf(-v)); v = b2f(x[r * N + c]) + b2f(s[r * N + c]) * g; }
    C[r * N + c] = f2b(v);
  }
}
__global__ void k_empty() {}
extern "C" void kernel_launch(void* const* d_in, const int* in_sizes, int n_in, void* d_out, int out_size, void* d_ws, size_t ws_size, hipStream_t stream) {
  const u16* x = (const u16*)d_in[0];
  char* ws = (char*)d_ws;
  u16* xn = (u16*)ws; u16* xl = (u16*)(ws + (2 << 20)); u16* h = (u16*)(ws + (4 << 20)); u16* x1 = (u16*)(ws + (6 << 20));
  u16* s = (u16*)(ws + (8 << 20)); float* K = (float*)(ws + (10 << 20)); u16* h2 = (u16*)(ws + (16 << 20));
  k_ln<<<L / 4, 256, 0, stream>>>(x, xn, L);
  k_ln<<<L / 4, 256, 0, stream>>>(xn, xl, L);
  k_kgen<<<D, 256, 0, stream>>>((u16*)d_in[1], (u16*)d_in[2], (u16*)d_in[3], (u16*)d_in[4], (u16*)d_in[5], (u16*)d_in[6], (u16*)d_in[7], K);
  k_conv<<<D, 256, 0, stream>>>(xn, K, (u16*)d_in[8], s);
  k_gemm<0><<<dim3(D / 16, L / 64), 256, 0, stream>>>(xl, (u16*)d_in[10], h, D, D, nullptr, nullptr, nullptr);
  k_gemm<1><<<dim3(D / 16, L / 64), 256, 0, stream>>>(h, (u16*)d_in[11], x1, D, D, xn, nullptr, nullptr);
  k_ln<<<L / 4, 256, 0, stream>>>(x1, xl, L);
  k_gemm<2><<<dim3(F / 16, L / 64), 256, 0, stream>>>(xl, (u16*)d_in[12], h2, D, F, nullptr, nullptr, nullptr);
  k_gemm<3><<<dim3(D / 16, L / 64), 256, 0, stream>>>(h2, (u16*)d_in[13], (u16*)d_out, F, D, x1, x, s);
}
```

```cpp
#include <hip/hip_runtime.h>
typedef unsigned short u16;
typedef unsigned u32;
typedef unsigned long long u64;
typedef u32 v4u __attribute__((ext_vector_type(4)));
typedef u32 v2u __attribute__((ext_vector_type(2)));
typedef float f4 __attribute__((ext_vector_type(4)));
typedef float f2 __attribute__((ext_vector_type(2)));
typedef short s8v __attribute__((ext_vector_type(8)));
typedef short s4v __attribute__((ext_vector_type(4)));
typedef __bf16 bf2 __attribute__((ext_vector_type(2)));
#define LAS __attribute__((address_space(3)))
#define GAS __attribute__((address_space(1)))
#define RLX_AGENT __ATOMIC_RELAXED, __HIP_MEMORY_SCOPE_AGENT
#define DEV __device__ __forceinline__
#define SB() __builtin_amdgcn_sched_barrier(0)
#define CFENCE() asm volatile("" ::: "memory")
#define VMWAIT(n) asm volatile("s_waitcnt vmcnt(" #n ")" ::: "memory")
#define BAR() asm volatile("s_waitcnt lgkmcnt(0)\n\ts_barrier" ::: "memory")

constexpr u32 F_HF = 0, F_X1 = 2048, F_H2 = 4096, F_XT = 12288, F_S = 12544, F_W3 = 12800, F_W4 = 12832;
constexpr u32 FLAG_BYTES = 52224;
constexpr u32 O_HS = 65536, O_X1 = O_HS + 1835008, O_ST = O_X1 + 1835008, O_H2 = O_ST + 229376, O_XNT = O_H2 + 7340032,
              O_ST2 = O_XNT + 1835008, O_W3T = O_ST2 + 1835008, O_W4T = O_W3T + 1605632, O_CS3 = O_W4T + 1605632,
              O_DBG = O_CS3 + 8192, WS_END = O_DBG + (1 << 20);
constexpr u32 L_W1 = 0, L_W2 = 57344, L_C = 114688, FBS = 4608, L_XT = L_C + 8 * FBS, L_ST = L_XT + 9216, L_CS = L_ST + 1024;
constexpr u32 L_KC = 0, KCS = 4160, L_U = 33280, L_KT = 43520, KTS = 4128;
constexpr int AUX_SC1 = 16;
#define SPM 0
#define XPOLL_IT 5

#define STAMP(k)
#define FS(r, k)
#define CSTAMP(k)
#define DUMP()

DEV f4 mfma(v4u a, v4u b, f4 c) { return __builtin_amdgcn_mfma_f32_16x16x32_bf16(__builtin_bit_cast(s8v, a), __builtin_bit_cast(s8v, b), c, 0, 0, 0); }
DEV u32 pk(float a, float b) { return __builtin_bit_cast(u32, __builtin_convertvector((f2{a, b}), bf2)); }
DEV float asf(u32 v) { return __builtin_bit_cast(float, v); }
DEV u32 asu(float v) { return __builtin_bit_cast(u32, v); }
DEV float blo(u32 v) { return __builtin_bit_cast(float, v << 16); }
DEV float bhi(u32 v) { return __builtin_bit_cast(float, v & 0xffff0000u); }
DEV float gelu(float z) {
  const float c0 = -2.302208198f, c1 = -0.1029432397f;
  const float e = __builtin_amdgcn_exp2f(z * __builtin_fmaf(z * z, c1, c0));
  return z * __builtin_amdgcn_rcpf(1.0f + e);
}
DEV v2u trd(LAS char* p) { return __builtin_bit_cast(v2u, __builtin_amdgcn_ds_read_tr16_b64_v4i16((LAS s4v*)p)); }
DEV v4u trfrag(LAS char* p, int o0, int o1) { const v2u a = trd(p + o0), b = trd(p + o1); return v4u{a.x, a.y, b.x, b.y}; }
DEV void dma(u32 ldsaddr, u32 voff, const void* sbase) {
  asm volatile("s_mov_b32 m0, %0\n\ts_nop 0\n\tglobal_load_lds_dwordx4 %1, %2" ::"s"(ldsaddr), "v"(voff), "s"(sbase));
}
DEV void dma_sc1(u32 ldsaddr, u32 voff, const void* sbase) {
  asm volatile("s_mov_b32 m0, %0\n\ts_nop 0\n\tglobal_load_lds_dwordx4 %1, %2 sc1" ::"s"(ldsaddr), "v"(voff), "s"(sbase));
}
DEV void wait_flags(GAS u32* p, bool need) {
#pragma clang loop unroll(disable)
  for (int spin = 0; spin < (1 << 16); ++spin) {
    const u32 v = __hip_atomic_load(p, RLX_AGENT);
    if (__all(v != 0u || !need)) break;
    __builtin_amdgcn_s_sleep(1);
  }
  __builtin_amdgcn_fence(__ATOMIC_ACQUIRE, "wavefront");
}
DEV void set_flag(GAS u32* p) { __hip_atomic_store(p, 1u, RLX_AGENT); }
DEV f2 cmul(f2 a, f2 b) {
  f2 r;
  asm("v_pk_mul_f32 %0, %1, %2 op_sel:[0,0] op_sel_hi:[0,1]\n\tv_pk_fma_f32 %0, %1, %2, %0 op_sel:[1,1,0] op_sel_hi:[1,0,1] neg_lo:[0,1,0]"
      : "=&v"(r) : "v"(a), "v"(b));
  return r;
}
DEV f2 cexp2(float er, float ph) {
  const float m = __builtin_amdgcn_exp2f(er), f = __builtin_amdgcn_fractf(ph);
  return f2{m * __builtin_amdgcn_cosf(f), m * __builtin_amdgcn_sinf(f)};
}
DEV int mod7(int v) { return v >= 7 ? v - 7 : v; }

__global__ void __launch_bounds__(512, 2)
fused_kernel(const u16* __restrict__ x, const u16* __restrict__ Are, const u16* __restrict__ Aim, const u16* __restrict__ Bre,
             const u16* __restrict__ Bim, const u16* __restrict__ Cre, const u16* __restrict__ Cim, const u16* __restrict__ ldt,
             const u16* __restrict__ dsk, const u16* __restrict__ w1, const u16* __restrict__ w2, const u16* __restrict__ w3,
             const u16* __restrict__ w4, u16* __restrict__ out, unsigned char* __restrict__ ws) {
  __shared__ __attribute__((aligned(16))) char smem[163840];
  LAS char* const S = (LAS char*)smem;
  const u32 sbase = (u32)(size_t)S;
  const int tid = threadIdx.x, lane = tid & 63, w = __builtin_amdgcn_readfirstlane(tid >> 6);
  const int c = lane & 15, q = lane >> 4, pa = (lane >> 2) & 3, pp = lane & 3;
  const int xcd = blockIdx.x & 7, rk = blockIdx.x >> 3;
  GAS u32* const flags = (GAS u32*)ws;
  const __amdgpu_buffer_rsrc_t rs = __builtin_amdgcn_make_buffer_rsrc(ws, 0, WS_END, 0x00020000);
  const v4u ones = {0x3f803f80u, 0x3f803f80u, 0x3f803f80u, 0x3f803f80u};
  asm volatile("" ::"s"(x), "s"(Are), "s"(Aim), "s"(Bre), "s"(Bim), "s"(Cre), "s"(Cim), "s"(ldt), "s"(dsk), "s"(w1), "s"(w2), "s"(w3), "s"(w4), "s"(out), "s"(ws));
  CSTAMP(30);
  STAMP(0);

  if (rk < 4) {
    const int sp = xcd * 4 + rk;
    STAMP(4 + SPM);
    if (sp >= 28 || w == 7) return;
    const u32 lb = w * 16384;
    const u32 v3 = (lane >> 3) * 3584 + (lane & 7) * 16, v4 = (lane >> 3) * 896 + (lane & 7) * 16;
    const char* b3 = (const char*)w3 + w * 64 * 3584 + sp * 128;
    const char* b4 = (const char*)w4 + sp * 64 * 896 + w * 128;
#pragma unroll
    for (int g = 0; g < 8; ++g) dma(sbase + lb + ((2 * g & 7) + (g >> 2)) * 1024, v3, b3 + ((2 * g & 7) + (g >> 2)) * 8 * 3584);
    {
      LAS char* p = S + lb + (16 * q + pa) * 128 + 16 * pp;
      f4 cs[4] = {{0, 0, 0, 0}, {0, 0, 0, 0}, {0, 0, 0, 0}, {0, 0, 0, 0}};
#pragma unroll
      for (int u = 0; u < 2; ++u) {
        VMWAIT(4);
        if (u == 0) STAMP(1 + SPM);
#pragma unroll
        for (int et = 0; et < 4; ++et) {
          const int o = u * 1024 + 64 * (et >> 1) + 8 * (et & 1);
          const v4u a = trfrag(p, o, o + 512);
          __builtin_amdgcn_raw_buffer_store_b128(a, rs, lane * 16, O_W3T + (((2 * sp + (et >> 1)) * 14 + 2 * w + u) * 2 + (et & 1)) * 1024, AUX_SC1);
          cs[et] = mfma(a, ones, cs[et]);
        }
      }
#pragma unroll
      for (int et = 0; et < 4; ++et)
        if (c == 0) *(LAS f4*)(S + 114688 + w * 256 + (32 * (et >> 1) + 8 * q + 4 * (et & 1)) * 4) = cs[et];
    }
    BAR();
    if (w == 0) {
      float s = 0.f;
#pragma unroll
      for (int k = 0; k < 7; ++k) s += *(LAS float*)(S + 114688 + k * 256 + lane * 4);
      __builtin_amdgcn_raw_buffer_store_b32(__builtin_bit_cast(u32, s), rs, lane * 4, O_CS3 + sp * 256, AUX_SC1);
    }
    VMWAIT(0);
    BAR();
    if (tid == 0) set_flag(flags + F_W3 + sp);
    STAMP(2 + SPM);
#pragma unroll
    for (int g = 0; g < 8; ++g) dma(sbase + lb + 8192 + g * 1024, v4, b4 + g * 8 * 896);
    VMWAIT(0);
    v4u a4[8];
    {
      LAS char* p = S + lb + 8192 + (8 * q + pa) * 128 + 32 * pp;
#pragma unroll
      for (int e = 0; e < 2; ++e)
#pragma unroll
        for (int t = 0; t < 4; ++t) a4[e * 4 + t] = trfrag(p, e * 4096 + 8 * t, e * 4096 + 512 + 8 * t);
    }
#pragma unroll
    for (int e = 0; e < 2; ++e) {
      const int fb = 2 * sp + e;
#pragma unroll
      for (int t = 0; t < 4; ++t)
        __builtin_amdgcn_raw_buffer_store_b128(a4[e * 4 + t], rs, lane * 16, O_W4T + (((w * 8 + (fb & 7)) * 7 + (fb >> 3)) * 4 + t) * 1024, AUX_SC1);
    }
    VMWAIT(0);
    BAR();
    if (tid == 0) set_flag(flags + F_W4 + sp);
    STAMP(3 + SPM);
    DUMP();
    return;
  }

  const int cq = (rk - 4) / 7, j = (rk - 4) - 7 * cq, cl = xcd * 4 + cq;
  const int ti = w >> 1, u = w & 1, T = cl * 4 + ti, tok = T * 16 + c;
  const int ch = w >> 2, dch = 64 * j + 2 * cl + ch;

  int blkof[7];
#pragma unroll
  for (int b = 0; b < 7; ++b) blkof[b] = mod7(j + b);
  v4u X[7];
  const u32 xo = tok * 896 + 32 * q + 16 * u;
  u32 wo;
  {
    const int rho = 8 * w + (lane >> 3), fsw = ((rho >> 1) & 1) | (((rho >> 4) & 1) << 1);
    wo = rho * 896 + 128 * j + 16 * ((lane & 7) ^ fsw);
  }
#define LDX_(b) asm volatile("global_load_dwordx4 %0, %1, %2" : "=&v"(X[b]) : "v"(xo), "s"((const char*)x + blkof[b] * 128))
#define LDX(b) LDX_(b)
#define LDW(W, LO, o) LDW_(W, LO, o)
#define LDW_(W, LO, o) do { const int bk_ = mod7(2 * cq + (o)); dma(sbase + (LO) + mod7(bk_ + 7 - j) * 8192 + w * 1024, wo, (const char*)(W) + bk_ * 57344); } while (0)
  if (w < 6) {
    const u16* pb = w == 0 ? Are : w == 1 ? Aim : w == 2 ? Bre : w == 3 ? Bim : w == 4 ? Cre : Cim;
    dma(sbase + L_XT + w * 1024, lane * 16, (const char*)pb + (64 * j + 2 * cl) * 512);
  }
  FS(1, 1);
  LDX(0); LDW(w1, L_W1, 0); LDW(w1, L_W1, 1); LDW(w2, L_W2, 0);
  FS(1, 2);
  const u32 ldw = ((const u32*)ldt)[dch >> 1], dkw = ((const u32*)dsk)[dch >> 1];
  const float dskip = (dch & 1) ? bhi(dkw) : blo(dkw);
  u32 wa[4];
  {
    const int fsw = (pa >> 1) | ((q & 1) << 1);
    const u32 base = (16 * q + 8 * u + pa) * 128 + (((2 * pp) ^ fsw) << 4) + ((pp & 1) << 3);
#pragma unroll
    for (int p = 0; p < 4; ++p) {
      const int t = p ^ (2 * u) ^ (ti & 1);
      wa[p] = base ^ ((t >> 1) << 4) ^ ((t & 1) << 3);
    }
  }
  const bool sg = ((ti ^ q) & 1) != 0;
  u32 hoff;
  {
    const int g = (64 * j + 16 * q + 8 * u) / 112, sh = (g & 1) ? -1 - (g >> 1) : 1 + (g >> 1);
    const int t2 = tok + sh, tw = t2 & 2047;
    hoff = (tw >> 4) * 14336 + q * 256 + (tw & 15) * 16 + (t2 == tw ? 0u : 0x80000000u);
  }
  const float dt = blo(pk(expf((dch & 1) ? bhi(ldw) : blo(ldw)), 0.f));
  asm volatile("" : "+v"(wa[0]), "+v"(wa[1]), "+v"(wa[2]), "+v"(wa[3]), "+v"(hoff));

  f4 kp[3];
  {
    FS(1, 3);
    VMWAIT(4);
    FS(1, 4);
    BAR();
    FS(1, 5);
    STAMP(1);
    LAS u16* pl = (LAS u16*)(S + L_XT + ch * 512 + (64 * (w & 3) + lane) * 2);
    const float are = blo(pl[0]), aim = blo(pl[512]), bre = blo(pl[1024]), bim = blo(pl[1536]), cre = blo(pl[2048]), cim = blo(pl[2560]);
    LDX(1); LDX(2);
    const float er = dt * are * 1.4426950409f, ph = dt * aim * 0.15915494309f;
    const f2 cb = cmul(f2{cre, -cim}, f2{dt * bre, -dt * bim});
    LAS char* fw = S + L_C + w * FBS + lane * 4;
    LAS char* fr = S + L_C + w * FBS + c * 288 + q * 16;
#define PUT(row, z) do { const f2 z_ = (z); *(LAS u32*)(fw + (row) * 288) = pk(z_.x, z_.y); } while (0)
    const f2 p1 = cexp2(er, ph), p2 = cmul(p1, p1), p4 = cmul(p2, p2), p8 = cmul(p4, p4), p16 = cmul(p8, p8);
    const f2 g1 = cexp2(er * 256.f, ph * 256.f);
    FS(1, 6); SB(); LDX(3); SB();
    {
      const f2 wc = {p16.x, -p16.y}, wc2 = cmul(wc, wc);
      f2 r0 = cb, r1 = cmul(cb, wc);
#pragma unroll
      for (int i = 0; i < 8; ++i) {
        PUT(2 * i, r0); PUT(2 * i + 1, r1);
        if (i < 7) { r0 = cmul(r0, wc2); r1 = cmul(r1, wc2); }
        if (i == 2) { SB(); LDX(4); SB(); }
        if (i == 5) { SB(); LDX(5); SB(); }
      }
    }
    v4u A[4], B[4];
#pragma unroll
    for (int s = 0; s < 4; ++s) A[s] = *(LAS v4u*)(fr + s * 64);
    FS(1, 7); CFENCE(); LDX(6); SB();
    {
      f2 z = p1, y = p2;
#pragma unroll
      for (int i = 0; i < 4; ++i) {
        const f2 z2 = cmul(z, p2);
        PUT(2 * i, z); PUT(2 * i + 1, z2); PUT(8 + i, y); PUT(12 + i, cmul(y, g1));
        if (i < 3) { z = cmul(z, p4); y = cmul(y, p4); }
        if (i == 1) { SB(); LDW(w1, L_W1, 2); SB(); }
      }
    }
#pragma unroll
    for (int s = 0; s < 4; ++s) B[s] = *(LAS v4u*)(fr + s * 64);
    FS(1, 8); CFENCE(); LDW(w1, L_W1, 3); SB();
    const f2 g2 = cmul(g1, g1), g3 = cmul(g2, g1), p12 = cmul(p4, p8);
    PUT(0, p4); PUT(1, p12); PUT(2, cmul(p4, g1)); PUT(3, cmul(p12, g1)); PUT(4, cmul(p4, g2)); PUT(5, cmul(p12, g2));
    PUT(6, cmul(p4, g3)); PUT(7, cmul(p12, g3));
    kp[0] = f4{0, 0, 0, 0};
#pragma unroll
    for (int s = 0; s < 4; ++s) kp[0] = mfma(A[s], B[s], kp[0]);
    SB(); LDW(w1, L_W1, 4); SB();
    PUT(8, (f2{1.f, 0.f})); PUT(9, p8); PUT(10, g1); PUT(11, cmul(p8, g1)); PUT(12, g2); PUT(13, cmul(p8, g2)); PUT(14, g3); PUT(15, cmul(p8, g3));
#pragma unroll
    for (int s = 0; s < 4; ++s) B[s] = *(LAS v4u*)(fr + s * 64);
    FS(1, 9); CFENCE(); LDW(w1, L_W1, 5); SB();
    {
      f2 z = cmul(g2, g2);
#pragma unroll
      for (int i = 0; i < 4; ++i) { PUT(2 * i, z); PUT(2 * i + 1, cmul(z, p8)); z = cmul(z, g1); }
      PUT(8, z);
    }
    kp[1] = f4{0, 0, 0, 0};
#pragma unroll
    for (int s = 0; s < 4; ++s) kp[1] = mfma(A[s], B[s], kp[1]);
    SB(); LDW(w1, L_W1, 6); SB();
#pragma unroll
    for (int s = 0; s < 4; ++s) B[s] = *(LAS v4u*)(fr + s * 64);
    kp[2] = f4{0, 0, 0, 0};
#pragma unroll
    for (int s = 0; s < 4; ++s) kp[2] = mfma(A[s], B[s], kp[2]);
    CFENCE();
#undef PUT
  }
  FS(1, 10);
  STAMP(2);

  v4u xnp;
  {
    asm volatile("s_waitcnt vmcnt(5)" : "+v"(X[0]), "+v"(X[1]), "+v"(X[2]), "+v"(X[3]), "+v"(X[4]), "+v"(X[5]), "+v"(X[6])::"memory");
    STAMP(3); FS(1, 11);
    f4 G = {0, 0, 0, 0}, Sx = {0, 0, 0, 0};
#pragma unroll
    for (int b = 0; b < 7; ++b) { G = mfma(X[b], X[b], G); Sx = mfma(ones, X[b], Sx); }
    const float gd = (c & 2) ? ((c & 1) ? G[3] : G[2]) : ((c & 1) ? G[1] : G[0]);
    if (q == (c >> 2)) *(LAS f2*)(S + L_ST + (ti * 16 + c) * 16 + u * 8) = f2{Sx[0], gd};
    FS(1, 12);
    LDW(w2, L_W2, 1); LDW(w2, L_W2, 2);
    VMWAIT(2);
    FS(1, 13);
    BAR();
    FS(1, 14);
    STAMP(4);
    f4 acc[4] = {{0, 0, 0, 0}, {0, 0, 0, 0}, {0, 0, 0, 0}, {0, 0, 0, 0}}, cs = {0, 0, 0, 0};
    const bool hi2 = (ti & 2) != 0;
    v4u A[2][4];
#pragma unroll
    for (int p = 0; p < 4; ++p) A[0][p] = trfrag(S + L_W1 + wa[p], 0, 512);
#pragma unroll
    for (int b = 0; b < 7; ++b) {
      if (b < 6) {
#pragma unroll
        for (int p = 0; p < 4; ++p) A[(b + 1) & 1][p] = trfrag(S + L_W1 + wa[p], (b + 1) * 8192, (b + 1) * 8192 + 512);
      }
      if (b < 4) LDW(w2, L_W2, 3 + b);
      SB();
#pragma unroll
      for (int p = 0; p < 4; ++p) acc[p] = mfma(A[b & 1][p], X[b], acc[p]);
      { const v4u s0 = A[b & 1][0], s2 = A[b & 1][2];
        cs = mfma(v4u{hi2 ? s2.x : s0.x, hi2 ? s2.y : s0.y, hi2 ? s2.z : s0.z, hi2 ? s2.w : s0.w}, ones, cs); }
      SB();
      FS(1, 15 + b);
    }
    *(LAS f4*)(S + L_C + w * FBS + lane * 16) = acc[2];
    *(LAS f4*)(S + L_C + w * FBS + 1024 + lane * 16) = acc[3];
    if (c == 0) *(LAS f4*)(S + L_CS + u * 256 + ((ti & 2) ^ (2 * u) ^ (ti & 1)) * 64 + q * 16) = cs;
    FS(1, 22);
    BAR();
    FS(1, 23);
    STAMP(5);
    const f4 st = *(LAS f4*)(S + L_ST + (ti * 16 + c) * 16);
    const f4 o0 = *(LAS f4*)(S + L_C + (w ^ 1) * FBS + lane * 16), o1 = *(LAS f4*)(S + L_C + (w ^ 1) * FBS + 1024 + lane * 16);
    const int t0 = 2 * u + (ti & 1);
    const f4 c0 = *(LAS f4*)(S + L_CS + t0 * 64 + q * 16) + *(LAS f4*)(S + L_CS + 256 + t0 * 64 + q * 16);
    const f4 c1 = *(LAS f4*)(S + L_CS + (t0 ^ 1) * 64 + q * 16) + *(LAS f4*)(S + L_CS + 256 + (t0 ^ 1) * 64 + q * 16);
    const float mean = (st[0] + st[2]) * (1.f / 448.f);
    const float var = __builtin_fmaxf((st[1] + st[3]) * (1.f / 448.f) - mean * mean, 0.f);
    const float r1 = __builtin_amdgcn_rsqf(var + 1e-5f);
    const float rs2 = r1 * __builtin_amdgcn_rsqf(var * r1 * r1 + 1e-5f), nm = -mean * rs2;
    float h[8];
#pragma unroll
    for (int r = 0; r < 4; ++r) {
      h[r] = gelu(__builtin_fmaf(acc[0][r] + o0[r], rs2, nm * c0[r]));
      h[4 + r] = gelu(__builtin_fmaf(acc[1][r] + o1[r], rs2, nm * c1[r]));
    }
    const u32 a0 = pk(h[0], h[1]), a1 = pk(h[2], h[3]), b0 = pk(h[4], h[5]), b1 = pk(h[6], h[7]);
    v4u hv = {sg ? b0 : a0, sg ? b1 : a1, sg ? a0 : b0, sg ? a1 : b1};
    if ((int)hoff < 0) hv = v4u{0, 0, 0, 0};
    __builtin_amdgcn_raw_buffer_store_b128(hv, rs, hoff & 0x7fffffffu, O_HS + (2 * j + u) * 1024, AUX_SC1);
    STAMP(6); FS(1, 24);
    const v4u xpc = X[0];
    *(LAS v4u*)(S + L_C + w * FBS + 3072 + lane * 16) = xpc;
    const float m1 = -mean * r1;
#pragma unroll
    for (int k = 0; k < 4; ++k) xnp[k] = pk(__builtin_fmaf(blo(xpc[k]), r1, m1), __builtin_fmaf(bhi(xpc[k]), r1, m1));
  }

  v4u W3[28];
#define LDW3(k) W3[k] = __builtin_amdgcn_raw_buffer_load_b128(rs, lane * 16 + ((k) & 3) * 1024, O_W3T + (8 * j + w) * 28672 + ((k) >> 2) * 4096, AUX_SC1)
  {
    LAS char* xt = S + L_XT + (16 * q + 8 * u) * 144 + (16 * ti + c) * 2;
#pragma unroll
    for (int k = 0; k < 4; ++k) {
      *(LAS u16*)(xt + (2 * k) * 144) = (u16)xnp[k];
      *(LAS u16*)(xt + (2 * k + 1) * 144) = (u16)(xnp[k] >> 16);
    }
    BAR();
    const v4u tv = *(LAS v4u*)(S + L_XT + (8 * w + (lane >> 3)) * 144 + (lane & 7) * 16);
#pragma unroll
    for (int k = 0; k < 3; ++k) *(LAS f4*)(S + L_C + w * FBS + (16 * k + c) * 64 + q * 16) = kp[k];
    if (tid < 32) *(LAS v4u*)(S + L_KC + (tid >> 2) * KCS + 4096 + (tid & 3) * 16) = v4u{0, 0, 0, 0};
    else if (tid < 160) *(LAS v4u*)(S + L_U + ((tid - 32) >> 4) * 1280 + (tid & 15) * 16) = v4u{0, 0, 0, 0};
    else if (tid < 162) *(LAS v2u*)(S + L_KT + (tid - 160) * KTS) = v2u{0, 0};
    VMWAIT(0);
    if (lane == 0) set_flag(flags + F_HF + (u * 128 + T) * 8 + j);
    STAMP(7);
    __builtin_amdgcn_raw_buffer_store_b128(tv, rs, (64 * j + 8 * w + (lane >> 3)) * 4096 + (lane & 7) * 16, O_XNT + cl * 128, AUX_SC1);
    BAR();
    const int tp = tid & 255;
    if (tp < 164) {
      LAS char* pp4 = S + L_C + ch * 4 * FBS + tp * 16;
      const f4 v = (*(LAS f4*)(pp4) + *(LAS f4*)(pp4 + FBS)) + (*(LAS f4*)(pp4 + 2 * FBS) + *(LAS f4*)(pp4 + 3 * FBS));
      const int col = tp >> 2;
      int cv;
      if (col < 8) cv = 2 * col + 1;
      else if (col < 16) cv = 2 + 4 * (col & 3) + 256 * ((col - 8) >> 2);
      else if (col < 24) cv = 4 + 8 * (col & 1) + 256 * ((col - 16) >> 1);
      else cv = 8 * (col & 1) + 256 * ((col - 24) >> 1);
      LAS char* kt = S + L_KT + ch * KTS + 8 + cv * 2 + (tp & 3) * 128;
      const u32 k01 = pk(v[0], v[1]), k23 = pk(v[2], v[3]);
      if (tp <= 160) *(LAS u16*)(kt) = (u16)k01;
      if (tp < 160) { *(LAS u16*)(kt + 32) = (u16)(k01 >> 16); *(LAS u16*)(kt + 64) = (u16)k23; *(LAS u16*)(kt + 96) = (u16)(k23 >> 16); }
    }
    BAR();
    {
      const float m0 = (float)(2047 - 8 * tp);
      u32 v[12];
#pragma unroll
      for (int e = 0; e < 12; e += 2) {
        const u32 r = pk(m0 - (float)e, m0 - (float)(e + 1));
        v[e] = *(LAS u16*)(S + L_KT + ch * KTS + 8 + 2 * (int)blo(r));
        if (e < 10) v[e + 1] = *(LAS u16*)(S + L_KT + ch * KTS + 8 + 2 * (int)bhi(r));
      }
      u32 pr[10];
#pragma unroll
      for (int e = 0; e < 10; ++e) pr[e] = v[e] | (v[e + 1] << 16);
#pragma unroll
      for (int k = 0; k < 4; ++k) *(LAS v4u*)(S + L_KC + (ch * 4 + k) * KCS + tp * 16) = v4u{pr[3 - k], pr[5 - k], pr[7 - k], pr[9 - k]};
    }
    VMWAIT(0);
    BAR();
    if (tid == 0) set_flag(flags + F_XT + j * 32 + cl);
    STAMP(8);
  }

  f4 cs3a, cs3b;
  {
    v4u A[28];
#pragma unroll
    for (int b = 0; b < 7; ++b) {
#pragma unroll
      for (int p = 0; p < 4; ++p) A[4 * b + p] = trfrag(S + L_W2 + wa[p], b * 8192, b * 8192 + 512);
      SB();
    }
    {
      const int jj = lane & 7, dt_ = lane >> 3;
      wait_flags(dt_ < 3 ? flags + F_HF + (u * 128 + ((T + dt_ - 1) & 127)) * 8 + (jj < 7 ? jj : 6) : flags + F_W3 + 4 * j + ti, true);
    }
    STAMP(9);
    v4u H[7];
#pragma unroll
    for (int b = 0; b < 7; ++b) H[b] = __builtin_amdgcn_raw_buffer_load_b128(rs, lane * 16, O_HS + (T * 14 + 2 * blkof[b] + u) * 1024, AUX_SC1);
    SB();
    f4 acc[4] = {{0, 0, 0, 0}, {0, 0, 0, 0}, {0, 0, 0, 0}, {0, 0, 0, 0}};
#pragma unroll
    for (int b = 0; b < 7; ++b) {
#pragma unroll
      for (int p = 0; p < 4; ++p) acc[p] = mfma(A[4 * b + p], H[b], acc[p]);
      LDW3(b);
      SB();
    }
    *(LAS f4*)(S + L_C + w * FBS + lane * 16) = acc[2];
    *(LAS f4*)(S + L_C + w * FBS + 1024 + lane * 16) = acc[3];
    BAR();
    STAMP(10);
    const f4 o0 = *(LAS f4*)(S + L_C + (w ^ 1) * FBS + lane * 16), o1 = *(LAS f4*)(S + L_C + (w ^ 1) * FBS + 1024 + lane * 16);
    const u32 n0 = sg ? xnp[2] : xnp[0], n1 = sg ? xnp[3] : xnp[1], n2 = sg ? xnp[0] : xnp[2], n3 = sg ? xnp[1] : xnp[3];
    const u32 a0 = pk(acc[0][0] + o0[0] + blo(n0), acc[0][1] + o0[1] + bhi(n0)), a1 = pk(acc[0][2] + o0[2] + blo(n1), acc[0][3] + o0[3] + bhi(n1));
    const u32 b0 = pk(acc[1][0] + o1[0] + blo(n2), acc[1][1] + o1[1] + bhi(n2)), b1 = pk(acc[1][2] + o1[2] + blo(n3), acc[1][3] + o1[3] + bhi(n3));
    const v4u x1p = v4u{sg ? b0 : a0, sg ? b1 : a1, sg ? a0 : b0, sg ? a1 : b1};
    __builtin_amdgcn_raw_buffer_store_b128(x1p, rs, lane * 16, O_X1 + (T * 14 + 2 * j + u) * 1024, AUX_SC1);
    *(LAS v4u*)(S + L_C + w * FBS + 2048 + lane * 16) = x1p;
    float s1 = 0.f, s2 = 0.f;
#pragma unroll
    for (int k = 0; k < 4; ++k) {
      const float a = blo(x1p[k]), b = bhi(x1p[k]);
      s1 += a + b; s2 = __builtin_fmaf(a, a, __builtin_fmaf(b, b, s2));
    }
    s1 += __shfl_xor(s1, 16); s2 += __shfl_xor(s2, 16);
    s1 += __shfl_xor(s1, 32); s2 += __shfl_xor(s2, 32);
    if (q == 0) __builtin_amdgcn_raw_buffer_store_b64(v2u{__builtin_bit_cast(u32, s1), __builtin_bit_cast(u32, s2)}, rs, c * 8, O_ST + ((T * 2 + u) * 7 + j) * 128, AUX_SC1);
    STAMP(11);
  }

  {
    wait_flags(flags + F_XT + j * 32 + (lane & 31), true);
    VMWAIT(0);
    if (lane == 0) set_flag(flags + F_X1 + (T * 2 + u) * 8 + j);
    STAMP(12);
    const int tp = tid & 255;
    const v4u uv = __builtin_amdgcn_raw_buffer_load_b128(rs, tp * 16, O_XNT + dch * 4096, AUX_SC1);
    SB();
    LDW3(7); LDW3(8);
    cs3a = __builtin_bit_cast(f4, __builtin_amdgcn_raw_buffer_load_b128(rs, q * 32, O_CS3 + (8 * j + w) * 128, AUX_SC1));
    cs3b = __builtin_bit_cast(f4, __builtin_amdgcn_raw_buffer_load_b128(rs, q * 32 + 16, O_CS3 + (8 * j + w) * 128, AUX_SC1));
    SB();
    *(LAS v4u*)(S + L_U + ch * 5120 + (tp & 3) * 1280 + 256 + (tp >> 2) * 16) = uv;
    BAR();
    STAMP(13);
    const int rho = (w >> 1) & 1;
    v2u sa, sb;
    LAS char* pa_ = S + L_KC + (ch * 4 + (c & 3)) * KCS + 2 * (2044 - (c & 12) + 8 * q - 16 * rho - 32 * u) - 3072 - 896;
    LAS char* pb_ = S + L_U + ch * 5120 + q * 1280 + (16 + c - u) * 16 - 224;
    f4 y[4] = {{0, 0, 0, 0}, {0, 0, 0, 0}, {0, 0, 0, 0}, {0, 0, 0, 0}};
    v4u B[2][4], A[2][4];
#define CONVLD(it) _Pragma("unroll") for (int a = 0; a < 4; ++a) { \
      B[(it) & 1][a] = *(LAS v4u*)(pb_ + (7 - (it)) * 32 + a * 256); \
      const v2u lo = *(volatile LAS v2u*)(pa_ + (7 - (it)) * 128 + (3 - a) * 1024), hi = *(volatile LAS v2u*)(pa_ + (7 - (it)) * 128 + (3 - a) * 1024 + 8); \
      A[(it) & 1][a] = v4u{lo.x, lo.y, hi.x, hi.y}; }
    CONVLD(0);
#pragma unroll
    for (int it = 0; it < 8; ++it) {
      if (it < 7) { CONVLD(it + 1); }
      LDW3(9 + 2 * it); LDW3(10 + 2 * it);
      SB();
#pragma unroll
      for (int a = 0; a < 4; ++a)
#pragma unroll
        for (int k = a; k < 4; ++k) y[k] = mfma(A[it & 1][a], B[it & 1][k - a], y[k]);
      SB();
      if (it == XPOLL_IT) {
        wait_flags(lane < 8 ? flags + F_X1 + (T * 2 + u) * 8 + (lane < 7 ? lane : 6) : flags + F_W4 + 4 * ((lane & 7) < 7 ? (lane & 7) : 6) + ti, true);
        STAMP(15);
#pragma unroll
        for (int b = 0; b < 7; ++b) dma_sc1(sbase + L_W2 + (ti * 14 + 2 * b + u) * 1024, lane * 16, ws + O_X1 + (T * 14 + 2 * b + u) * 1024);
        sa = __builtin_amdgcn_raw_buffer_load_b64(rs, q * 128 + c * 8, O_ST + (T * 2 + u) * 896, AUX_SC1);
        sb = __builtin_amdgcn_raw_buffer_load_b64(rs, (q < 3 ? q + 4 : q) * 128 + c * 8, O_ST + (T * 2 + u) * 896, AUX_SC1);
        SB();
      }
    }
    LDW3(25); LDW3(26); LDW3(27);
    STAMP(14);
    SB();
    const f4 e0 = u ? y[0] : y[2], e1 = u ? y[1] : y[3], k0 = u ? y[2] : y[0], k1 = u ? y[3] : y[1];
    *(LAS f4*)(S + L_C + w * FBS + lane * 16) = e0;
    *(LAS f4*)(S + L_C + w * FBS + 1024 + lane * 16) = e1;
    BAR();
#pragma unroll
    for (int k = 0; k < 2; ++k) {
      const f4 v = (k ? k1 : k0) + *(LAS f4*)(S + L_C + (w ^ 1) * FBS + k * 1024 + lane * 16);
      const int blk = 16 * (2 * u + k) + c;
      const v2u uu = *(LAS v2u*)(S + L_U + ch * 5120 + (2 * rho + (q >> 1)) * 1280 + (16 + blk) * 16 + (q & 1) * 8);
      const v2u o = {pk(__builtin_fmaf(blo(uu.x), dskip, v[0]), __builtin_fmaf(bhi(uu.x), dskip, v[1])),
                     pk(__builtin_fmaf(blo(uu.y), dskip, v[2]), __builtin_fmaf(bhi(uu.y), dskip, v[3]))};
      *(LAS v2u*)(S + L_KT + ch * 4096 + blk * 64 + rho * 32 + q * 8) = o;
    }
    {
      float s1 = asf(sa.x) + (q < 3 ? asf(sb.x) : 0.f);
      float s2 = asf(sa.y) + (q < 3 ? asf(sb.y) : 0.f);
      s1 += __shfl_xor(s1, 16); s2 += __shfl_xor(s2, 16);
      s1 += __shfl_xor(s1, 32); s2 += __shfl_xor(s2, 32);
      if (q == 0) *(LAS f2*)(S + L_ST + (ti * 16 + c) * 16 + u * 8) = f2{s1, s2};
    }
    VMWAIT(0);
    BAR();
    const v4u sv = *(LAS v4u*)(S + L_KT + tid * 16);
    __builtin_amdgcn_raw_buffer_store_b128(sv, rs, tp * 16, O_ST2 + dch * 4096, AUX_SC1);
    STAMP(16);
  }

  v4u W4[28], own[4], H2[4][6];
  const u32 w4b = O_W4T + (j * 8 + w) * 28672;
#define LDW4(k) W4[k] = __builtin_amdgcn_raw_buffer_load_b128(rs, lane * 16 + ((k) & 3) * 1024, w4b + blkof[(k) >> 2] * 4096, AUX_SC1)
#define LDH2(s) _Pragma("unroll") for (int b = 1; b < 7; ++b) \
    H2[s][b - 1] = __builtin_amdgcn_raw_buffer_load_b128(rs, lane * 16, O_H2 + ((cl * 4 + ((ti + 1 + (s)) & 3)) * 56 + blkof[b] * 8 + w) * 1024, AUX_SC1)
#define WAITH2(s) wait_flags(flags + F_H2 + ((cl * 4 + ((ti + 1 + (s)) & 3)) * 8 + w) * 8 + (lane & 7), (lane & 7) < 7 && (lane & 7) != j)
  if (w < 4) __builtin_amdgcn_s_setprio(1);
  v4u Bq[4];
#pragma unroll
  for (int k = 0; k < 3; ++k) Bq[k] = *(LAS v4u*)(S + L_W2 + ((ti + 1) & 3) * 14336 + lane * 16 + k * 1024);
#pragma unroll
  for (int s = 0; s < 4; ++s) {
    const int tl = (ti + 1 + s) & 3;
    LAS char* ap = S + L_W2 + tl * 14336 + lane * 16;
    LAS char* an = S + L_W2 + ((tl + 1) & 3) * 14336 + lane * 16;
    f4 a0 = {0, 0, 0, 0}, a1 = {0, 0, 0, 0};
#pragma unroll
    for (int ks = 0; ks < 14; ++ks) {
      const int qi = (s * 14 + ks) & 3, qn = (s * 14 + ks + 3) & 3;
      if (ks + 3 < 14) Bq[qn] = *(LAS v4u*)(ap + (ks + 3) * 1024);
      else if (s < 3) Bq[qn] = *(LAS v4u*)(an + (ks - 11) * 1024);
      if (s < 3 && ks % 3 == 0 && s * 5 + ks / 3 < 14) LDW4(s * 5 + ks / 3);
      SB();
      a0 = mfma(W3[2 * ks], Bq[qi], a0);
      a1 = mfma(W3[2 * ks + 1], Bq[qi], a1);
      if (s == 3) LDW4(14 + ks);
      SB();
    }
    if (s == 0) {
      VMWAIT(5);
      BAR();
      if (tid == 0) set_flag(flags + F_S + j * 32 + cl);
    } else {
      if (s == 1) VMWAIT(5);
      if (s == 2) VMWAIT(4);
      if (s == 3) VMWAIT(20);
      if (lane == 0) set_flag(flags + F_H2 + ((cl * 4 + ((ti + s) & 3)) * 8 + w) * 8 + j);
    }
    const f4 st = *(LAS f4*)(S + L_ST + (tl * 16 + c) * 16);
    const float mean = (st[0] + st[2]) * (1.f / 448.f);
    const float rstd = __builtin_amdgcn_rsqf(__builtin_fmaxf((st[1] + st[3]) * (1.f / 448.f) - mean * mean, 0.f) + 1e-5f), nm = -mean * rstd;
    float h[8];
#pragma unroll
    for (int r = 0; r < 4; ++r) {
      h[r] = gelu(__builtin_fmaf(a0[r], rstd, nm * cs3a[r]));
      h[4 + r] = gelu(__builtin_fmaf(a1[r], rstd, nm * cs3b[r]));
    }
    own[s] = v4u{pk(h[0], h[1]), pk(h[2], h[3]), pk(h[4], h[5]), pk(h[6], h[7])};
    __builtin_amdgcn_raw_buffer_store_b128(own[s], rs, lane * 16, O_H2 + ((cl * 4 + tl) * 56 + j * 8 + w) * 1024, AUX_SC1);
    SB();
    if (s == 2) { WAITH2(0); LDH2(0); SB(); }
    STAMP(17 + s);
  }
  f4 o[4][4];
#pragma unroll
  for (int s = 0; s < 4; ++s)
#pragma unroll
    for (int t = 0; t < 4; ++t) o[s][t] = mfma(W4[t], own[s], f4{0, 0, 0, 0});
  SB();
  WAITH2(1);
  VMWAIT(0);
  if (lane == 0) set_flag(flags + F_H2 + ((cl * 4 + ti) * 8 + w) * 8 + j);
  LDH2(1);
  SB();
  STAMP(21);
  f4 pre0 = {0, 0, 0, 0}, pre1 = {0, 0, 0, 0};
#pragma unroll
  for (int s = 0; s < 4; ++s) {
    const int tl = (ti + 1 + s) & 3;
    if (s == 3) {
#pragma unroll
      for (int k = 0; k < 6; ++k) {
        pre0 += *(LAS f4*)(S + (ti * 7 + k) * 4096 + (2 * u) * 1024 + lane * 16);
        pre1 += *(LAS f4*)(S + (ti * 7 + k) * 4096 + (2 * u + 1) * 1024 + lane * 16);
      }
    }
#pragma unroll
    for (int b = 1; b < 7; ++b)
#pragma unroll
      for (int t = 0; t < 4; ++t) o[s][t] = mfma(W4[4 * b + t], H2[s][b - 1], o[s][t]);
    SB();
    if (s < 2) { WAITH2(s + 2); LDH2(s + 2); SB(); }
    if (s == 0) BAR();
    if (s < 3) {
#pragma unroll
      for (int t = 0; t < 4; ++t) *(LAS f4*)(S + (tl * 7 + 2 * s + u) * 4096 + t * 1024 + lane * 16) = o[s][t];
    }
    if (s == 1) {
      wait_flags(flags + F_S + j * 32 + (lane & 31), true);
      dma_sc1(sbase + L_XT + w * 1024, (lane >> 3) * 4096 + (lane & 7) * 16, ws + O_ST2 + (64 * j + 8 * w) * 4096 + cl * 128);
    }
    if (s == 2) { VMWAIT(0); BAR(); }
    STAMP(22 + s);
  }
  {
    const f4 e0 = u ? o[3][0] : o[3][2], e1 = u ? o[3][1] : o[3][3], k0 = u ? o[3][2] : o[3][0], k1 = u ? o[3][3] : o[3][1];
    *(LAS f4*)(S + (ti * 7 + 6) * 4096 + (2 * (u ^ 1)) * 1024 + lane * 16) = e0;
    *(LAS f4*)(S + (ti * 7 + 6) * 4096 + (2 * (u ^ 1) + 1) * 1024 + lane * 16) = e1;
    const v4u xpc = *(LAS v4u*)(S + L_C + w * FBS + 3072 + lane * 16), x1p = *(LAS v4u*)(S + L_C + w * FBS + 2048 + lane * 16);
    const v4u sp_ = trfrag(S + L_XT + (16 * q + 8 * u + pa) * 128 + (16 * ti + 4 * pp) * 2, 0, 512);
    BAR();
    const f4 v0 = k0 + pre0 + *(LAS f4*)(S + (ti * 7 + 6) * 4096 + (2 * u) * 1024 + lane * 16);
    const f4 v1 = k1 + pre1 + *(LAS f4*)(S + (ti * 7 + 6) * 4096 + (2 * u + 1) * 1024 + lane * 16);
    float r[8];
#pragma unroll
    for (int k = 0; k < 8; ++k) {
      const float v = (k < 4 ? v0[k & 3] : v1[k & 3]) + ((k & 1) ? bhi(x1p[k >> 1]) : blo(x1p[k >> 1]));
      const float g = v * __builtin_amdgcn_rcpf(1.0f + __builtin_amdgcn_exp2f(v * -1.4426950409f));
      r[k] = __builtin_fmaf((k & 1) ? bhi(sp_[k >> 1]) : blo(sp_[k >> 1]), g, (k & 1) ? bhi(xpc[k >> 1]) : blo(xpc[k >> 1]));
    }
    *(GAS v4u*)((GAS char*)out + tok * 896 + 128 * j + 32 * q + 16 * u) = v4u{pk(r[0], r[1]), pk(r[2], r[3]), pk(r[4], r[5]), pk(r[6], r[7])};
    STAMP(26);
    DUMP();
  }
}

extern "C" void kernel_launch(void* const* d_in, const int* in_sizes, int n_in, void* d_out, int out_size, void* d_ws, size_t ws_size,
                              hipStream_t stream) {
  (void)hipMemsetAsync(d_ws, 0, FLAG_BYTES, stream);
  fused_kernel<<<256, 512, 0, stream>>>((const u16*)d_in[0], (const u16*)d_in[1], (const u16*)d_in[2], (const u16*)d_in[3], (const u16*)d_in[4],
                                        (const u16*)d_in[5], (const u16*)d_in[6], (const u16*)d_in[7], (const u16*)d_in[8], (const u16*)d_in[10],
                                        (const u16*)d_in[11], (const u16*)d_in[12], (const u16*)d_in[13], (u16*)d_out, (unsigned char*)d_ws);
}
```

```cpp
#define EVK 100
#include <hip/hip_runtime.h>
typedef unsigned short u16;
typedef unsigned u32;
typedef unsigned long long u64;
typedef u32 v4u __attribute__((ext_vector_type(4)));
typedef u32 v2u __attribute__((ext_vector_type(2)));
typedef float f4 __attribute__((ext_vector_type(4)));
typedef float f2 __attribute__((ext_vector_type(2)));
typedef short s8v __attribute__((ext_vector_type(8)));
typedef short s4v __attribute__((ext_vector_type(4)));
typedef __bf16 bf2 __attribute__((ext_vector_type(2)));
#define LAS __attribute__((address_space(3)))
#define GAS __attribute__((address_space(1)))
#define RLX_AGENT __ATOMIC_RELAXED, __HIP_MEMORY_SCOPE_AGENT
#define DEV __device__ __forceinline__
#define SB() __builtin_amdgcn_sched_barrier(0)
#define CFENCE() asm volatile("" ::: "memory")
#define VMWAIT(n) asm volatile("s_waitcnt vmcnt(" #n ")" ::: "memory")
#define BAR() asm volatile("s_waitcnt lgkmcnt(0)\n\ts_barrier" ::: "memory")

constexpr u32 F_HF = 0, F_X1 = 2048, F_H2 = 4096, F_XT = 12288, F_S = 12544, F_W3 = 12800, F_W4 = 12832;
constexpr u32 FLAG_BYTES = 52224;
constexpr u32 O_HS = 65536, O_X1 = O_HS + 1835008, O_ST = O_X1 + 1835008, O_H2 = O_ST + 229376, O_XNT = O_H2 + 7340032,
              O_ST2 = O_XNT + 1835008, O_W3T = O_ST2 + 1835008, O_W4T = O_W3T + 1605632, O_CS3 = O_W4T + 1605632,
              O_DBG = O_CS3 + 8192, WS_END = O_DBG + (1 << 20);
constexpr u32 L_W1 = 0, L_W2 = 57344, L_C = 114688, FBS = 4608, L_XT = L_C + 8 * FBS, L_ST = L_XT + 9216, L_CS = L_ST + 1024;
constexpr u32 L_KC = 0, KCS = 4160, L_U = 33280, L_KT = 43520, KTS = 4128;
constexpr int AUX_SC1 = 16;
#define SPM 0
#define XPOLL_IT 5

#define STAMP(k) EV(k)
#define FS(r, k)
#define CSTAMP(k)
#define DUMP()

DEV f4 mfma(v4u a, v4u b, f4 c) { return __builtin_amdgcn_mfma_f32_16x16x32_bf16(__builtin_bit_cast(s8v, a), __builtin_bit_cast(s8v, b), c, 0, 0, 0); }
DEV u32 pk(float a, float b) { return __builtin_bit_cast(u32, __builtin_convertvector((f2{a, b}), bf2)); }
DEV float asf(u32 v) { return __builtin_bit_cast(float, v); }
DEV u32 asu(float v) { return __builtin_bit_cast(u32, v); }
DEV float blo(u32 v) { return __builtin_bit_cast(float, v << 16); }
DEV float bhi(u32 v) { return __builtin_bit_cast(float, v & 0xffff0000u); }
DEV float gelu(float z) {
  const float c0 = -2.302208198f, c1 = -0.1029432397f;
  const float e = __builtin_amdgcn_exp2f(z * __builtin_fmaf(z * z, c1, c0));
  return z * __builtin_amdgcn_rcpf(1.0f + e);
}
DEV v2u trd(LAS char* p) { return __builtin_bit_cast(v2u, __builtin_amdgcn_ds_read_tr16_b64_v4i16((LAS s4v*)p)); }
DEV v4u trfrag(LAS char* p, int o0, int o1) { const v2u a = trd(p + o0), b = trd(p + o1); return v4u{a.x, a.y, b.x, b.y}; }
DEV void dma(u32 ldsaddr, u32 voff, const void* sbase) {
  asm volatile("s_mov_b32 m0, %0\n\ts_nop 0\n\tglobal_load_lds_dwordx4 %1, %2" ::"s"(ldsaddr), "v"(voff), "s"(sbase));
}
DEV void dma_sc1(u32 ldsaddr, u32 voff, const void* sbase) {
  asm volatile("s_mov_b32 m0, %0\n\ts_nop 0\n\tglobal_load_lds_dwordx4 %1, %2 sc1" ::"s"(ldsaddr), "v"(voff), "s"(sbase));
}
DEV void wait_flags(GAS u32* p, bool need) {
#pragma clang loop unroll(disable)
  for (int spin = 0; spin < (1 << 16); ++spin) {
    const u32 v = __hip_atomic_load(p, RLX_AGENT);
    if (__all(v != 0u || !need)) break;
    __builtin_amdgcn_s_sleep(1);
  }
  __builtin_amdgcn_fence(__ATOMIC_ACQUIRE, "wavefront");
}
DEV void set_flag(GAS u32* p) { __hip_atomic_store(p, 1u, RLX_AGENT); }
DEV f2 cmul(f2 a, f2 b) {
  f2 r;
  asm("v_pk_mul_f32 %0, %1, %2 op_sel:[0,0] op_sel_hi:[0,1]\n\tv_pk_fma_f32 %0, %1, %2, %0 op_sel:[1,1,0] op_sel_hi:[1,0,1] neg_lo:[0,1,0]"
      : "=&v"(r) : "v"(a), "v"(b));
  return r;
}
DEV f2 cexp2(float er, float ph) {
  const float m = __builtin_amdgcn_exp2f(er), f = __builtin_amdgcn_fractf(ph);
  return f2{m * __builtin_amdgcn_cosf(f), m * __builtin_amdgcn_sinf(f)};
}
DEV int mod7(int v) { return v >= 7 ? v - 7 : v; }

__global__ void __launch_bounds__(512, 2)
fused_kernel(const u16* x_, const u16* Are_, const u16* Aim_, const u16* Bre_, const u16* Bim_, const u16* Cre_, const u16* Cim_, const u16* ldt_,
             const u16* dsk_, const u16* w1_, const u16* w2_, const u16* w3_, const u16* w4_, u16* out_, unsigned char* ws_) {
  u64 tstart_; asm volatile("s_memrealtime %0\n s_waitcnt lgkmcnt(0)" : "=s"(tstart_) :: "memory");
  typedef u64 u64x8 __attribute__((ext_vector_type(8)));
  u64x8 ka0, ka1;
  asm volatile("s_load_dwordx16 %0, %2, 0x0\n s_load_dwordx16 %1, %2, 0x38\n s_waitcnt lgkmcnt(0)" : "=&s"(ka0), "=&s"(ka1) : "s"(__builtin_amdgcn_kernarg_segment_ptr()) : "memory");
  const u16 *x = (const u16*)ka0[0], *Are = (const u16*)ka0[1], *Aim = (const u16*)ka0[2], *Bre = (const u16*)ka0[3], *Bim = (const u16*)ka0[4], *Cre = (const u16*)ka0[5],
            *Cim = (const u16*)ka0[6], *ldt = (const u16*)ka1[0], *dsk = (const u16*)ka1[1], *w1 = (const u16*)ka1[2], *w2 = (const u16*)ka1[3], *w3 = (const u16*)ka1[4], *w4 = (const u16*)ka1[5];
  u16* out = (u16*)ka1[6]; unsigned char* ws = (unsigned char*)ka1[7];
  u64 tev_ = 0;
#define EV(k) do { if ((k) == EVK) { SB(); tev_ = __builtin_amdgcn_s_memrealtime(); SB(); } } while (0)
  EV(100);
  __shared__ __attribute__((aligned(16))) char smem[163840];
  LAS char* const S = (LAS char*)smem;
  const u32 sbase = (u32)(size_t)S;
  const int tid = threadIdx.x, lane = tid & 63, w = __builtin_amdgcn_readfirstlane(tid >> 6);
  const int c = lane & 15, q = lane >> 4, pa = (lane >> 2) & 3, pp = lane & 3;
  const int xcd = blockIdx.x & 7, rk = blockIdx.x >> 3;
  GAS u32* const flags = (GAS u32*)ws;
  const __amdgpu_buffer_rsrc_t rs = __builtin_amdgcn_make_buffer_rsrc(ws, 0, WS_END, 0x00020000);
  const v4u ones = {0x3f803f80u, 0x3f803f80u, 0x3f803f80u, 0x3f803f80u};
  asm volatile("" ::"s"(x), "s"(Are), "s"(Aim), "s"(Bre), "s"(Bim), "s"(Cre), "s"(Cim), "s"(ldt), "s"(dsk), "s"(w1), "s"(w2), "s"(w3), "s"(w4), "s"(out), "s"(ws));
  CSTAMP(30);
  STAMP(0);

  if (rk < 4) {
    const int sp = xcd * 4 + rk;
    STAMP(4 + SPM);
    if (sp >= 28 || w == 7) return;
    const u32 lb = w * 16384;
    const u32 v3 = (lane >> 3) * 3584 + (lane & 7) * 16, v4 = (lane >> 3) * 896 + (lane & 7) * 16;
    const char* b3 = (const char*)w3 + w * 64 * 3584 + sp * 128;
    const char* b4 = (const char*)w4 + sp * 64 * 896 + w * 128;
#pragma unroll
    for (int g = 0; g < 8; ++g) dma(sbase + lb + ((2 * g & 7) + (g >> 2)) * 1024, v3, b3 + ((2 * g & 7) + (g >> 2)) * 8 * 3584);
    {
      LAS char* p = S + lb + (16 * q + pa) * 128 + 16 * pp;
      f4 cs[4] = {{0, 0, 0, 0}, {0, 0, 0, 0}, {0, 0, 0, 0}, {0, 0, 0, 0}};
#pragma unroll
      for (int u = 0; u < 2; ++u) {
        VMWAIT(4);
        if (u == 0) STAMP(1 + SPM);
#pragma unroll
        for (int et = 0; et < 4; ++et) {
          const int o = u * 1024 + 64 * (et >> 1) + 8 * (et & 1);
          const v4u a = trfrag(p, o, o + 512);
          __builtin_amdgcn_raw_buffer_store_b128(a, rs, lane * 16, O_W3T + (((2 * sp + (et >> 1)) * 14 + 2 * w + u) * 2 + (et & 1)) * 1024, AUX_SC1);
          cs[et] = mfma(a, ones, cs[et]);
        }
      }
#pragma unroll
      for (int et = 0; et < 4; ++et)
        if (c == 0) *(LAS f4*)(S + 114688 + w * 256 + (32 * (et >> 1) + 8 * q + 4 * (et & 1)) * 4) = cs[et];
    }
    BAR();
    if (w == 0) {
      float s = 0.f;
#pragma unroll
      for (int k = 0; k < 7; ++k) s += *(LAS float*)(S + 114688 + k * 256 + lane * 4);
      __builtin_amdgcn_raw_buffer_store_b32(__builtin_bit_cast(u32, s), rs, lane * 4, O_CS3 + sp * 256, AUX_SC1);
    }
    VMWAIT(0);
    BAR();
    if (tid == 0) set_flag(flags + F_W3 + sp);
    STAMP(2 + SPM);
#pragma unroll
    for (int g = 0; g < 8; ++g) dma(sbase + lb + 8192 + g * 1024, v4, b4 + g * 8 * 896);
    VMWAIT(0);
    v4u a4[8];
    {
      LAS char* p = S + lb + 8192 + (8 * q + pa) * 128 + 32 * pp;
#pragma unroll
      for (int e = 0; e < 2; ++e)
#pragma unroll
        for (int t = 0; t < 4; ++t) a4[e * 4 + t] = trfrag(p, e * 4096 + 8 * t, e * 4096 + 512 + 8 * t);
    }
#pragma unroll
    for (int e = 0; e < 2; ++e) {
      const int fb = 2 * sp + e;
#pragma unroll
      for (int t = 0; t < 4; ++t)
        __builtin_amdgcn_raw_buffer_store_b128(a4[e * 4 + t], rs, lane * 16, O_W4T + (((w * 8 + (fb & 7)) * 7 + (fb >> 3)) * 4 + t) * 1024, AUX_SC1);
    }
    VMWAIT(0);
    BAR();
    if (tid == 0) set_flag(flags + F_W4 + sp);
    STAMP(3 + SPM);
    DUMP();
    return;
  }

  const int cq = (rk - 4) / 7, j = (rk - 4) - 7 * cq, cl = xcd * 4 + cq;
  const int ti = w >> 1, u = w & 1, T = cl * 4 + ti, tok = T * 16 + c;
  const int ch = w >> 2, dch = 64 * j + 2 * cl + ch;

  int blkof[7];
#pragma unroll
  for (int b = 0; b < 7; ++b) blkof[b] = mod7(j + b);
  v4u X[7];
  const u32 xo = tok * 896 + 32 * q + 16 * u;
  u32 wo;
  {
    const int rho = 8 * w + (lane >> 3), fsw = ((rho >> 1) & 1) | (((rho >> 4) & 1) << 1);
    wo = rho * 896 + 128 * j + 16 * ((lane & 7) ^ fsw);
  }
#define LDX_(b) asm volatile("global_load_dwordx4 %0, %1, %2" : "=&v"(X[b]) : "v"(xo), "s"((const char*)x + blkof[b] * 128))
#define LDX(b) LDX_(b)
#define LDW(W, LO, o) LDW_(W, LO, o)
#define LDW_(W, LO, o) do { const int bk_ = mod7(2 * cq + (o)); dma(sbase + (LO) + mod7(bk_ + 7 - j) * 8192 + w * 1024, wo, (const char*)(W) + bk_ * 57344); } while (0)
  if (w < 6) {
    const u16* pb = w == 0 ? Are : w == 1 ? Aim : w == 2 ? Bre : w == 3 ? Bim : w == 4 ? Cre : Cim;
    dma(sbase + L_XT + w * 1024, lane * 16, (const char*)pb + (64 * j + 2 * cl) * 512);
  }
  FS(1, 1);
  LDX(0); LDW(w1, L_W1, 0); LDW(w1, L_W1, 1); LDW(w2, L_W2, 0);
  FS(1, 2);
  const u32 ldw = ((const __attribute__((address_space(4))) u32*)ldt)[dch >> 1], dkw = ((const __attribute__((address_space(4))) u32*)dsk)[dch >> 1];
  const float dskip = (dch & 1) ? bhi(dkw) : blo(dkw);
  u32 wa[4];
  {
    const int fsw = (pa >> 1) | ((q & 1) << 1);
    const u32 base = (16 * q + 8 * u + pa) * 128 + (((2 * pp) ^ fsw) << 4) + ((pp & 1) << 3);
#pragma unroll
    for (int p = 0; p < 4; ++p) {
      const int t = p ^ (2 * u) ^ (ti & 1);
      wa[p] = base ^ ((t >> 1) << 4) ^ ((t & 1) << 3);
    }
  }
  const bool sg = ((ti ^ q) & 1) != 0;
  u32 hoff;
  {
    const int g = (64 * j + 16 * q + 8 * u) / 112, sh = (g & 1) ? -1 - (g >> 1) : 1 + (g >> 1);
    const int t2 = tok + sh, tw = t2 & 2047;
    hoff = (tw >> 4) * 14336 + q * 256 + (tw & 15) * 16 + (t2 == tw ? 0u : 0x80000000u);
  }
  const float dt = blo(pk(expf((dch & 1) ? bhi(ldw) : blo(ldw)), 0.f));
  asm volatile("" : "+v"(wa[0]), "+v"(wa[1]), "+v"(wa[2]), "+v"(wa[3]), "+v"(hoff));

  f4 kp[3];
  {
    FS(1, 3);
    VMWAIT(4);
    FS(1, 4);
    BAR();
    FS(1, 5);
    STAMP(1);
    LAS u16* pl = (LAS u16*)(S + L_XT + ch * 512 + (64 * (w & 3) + lane) * 2);
    const float are = blo(pl[0]), aim = blo(pl[512]), bre = blo(pl[1024]), bim = blo(pl[1536]), cre = blo(pl[2048]), cim = blo(pl[2560]);
    LDX(1); LDX(2);
    const float er = dt * are * 1.4426950409f, ph = dt * aim * 0.15915494309f;
    const f2 cb = cmul(f2{cre, -cim}, f2{dt * bre, -dt * bim});
    LAS char* fw = S + L_C + w * FBS + lane * 4;
    LAS char* fr = S + L_C + w * FBS + c * 288 + q * 16;
#define PUT(row, z) do { const f2 z_ = (z); *(LAS u32*)(fw + (row) * 288) = pk(z_.x, z_.y); } while (0)
    const f2 p1 = cexp2(er, ph), p2 = cmul(p1, p1), p4 = cmul(p2, p2), p8 = cmul(p4, p4), p16 = cmul(p8, p8);
    const f2 g1 = cexp2(er * 256.f, ph * 256.f);
    FS(1, 6); SB(); LDX(3); SB();
    {
      const f2 wc = {p16.x, -p16.y}, wc2 = cmul(wc, wc);
      f2 r0 = cb, r1 = cmul(cb, wc);
#pragma unroll
      for (int i = 0; i < 8; ++i) {
        PUT(2 * i, r0); PUT(2 * i + 1, r1);
        if (i < 7) { r0 = cmul(r0, wc2); r1 = cmul(r1, wc2); }
        if (i == 2) { SB(); LDX(4); SB(); }
        if (i == 5) { SB(); LDX(5); SB(); }
      }
    }
    v4u A[4], B[4];
#pragma unroll
    for (int s = 0; s < 4; ++s) A[s] = *(LAS v4u*)(fr + s * 64);
    FS(1, 7); CFENCE(); LDX(6); SB();
    {
      f2 z = p1, y = p2;
#pragma unroll
      for (int i = 0; i < 4; ++i) {
        const f2 z2 = cmul(z, p2);
        PUT(2 * i, z); PUT(2 * i + 1, z2); PUT(8 + i, y); PUT(12 + i, cmul(y, g1));
        if (i < 3) { z = cmul(z, p4); y = cmul(y, p4); }
        if (i == 1) { SB(); LDW(w1, L_W1, 2); SB(); }
      }
    }
#pragma unroll
    for (int s = 0; s < 4; ++s) B[s] = *(LAS v4u*)(fr + s * 64);
    FS(1, 8); CFENCE(); LDW(w1, L_W1, 3); SB();
    const f2 g2 = cmul(g1, g1), g3 = cmul(g2, g1), p12 = cmul(p4, p8);
    PUT(0, p4); PUT(1, p12); PUT(2, cmul(p4, g1)); PUT(3, cmul(p12, g1)); PUT(4, cmul(p4, g2)); PUT(5, cmul(p12, g2));
    PUT(6, cmul(p4, g3)); PUT(7, cmul(p12, g3));
    kp[0] = f4{0, 0, 0, 0};
#pragma unroll
    for (int s = 0; s < 4; ++s) kp[0] = mfma(A[s], B[s], kp[0]);
    SB(); LDW(w1, L_W1, 4); SB();
    PUT(8, (f2{1.f, 0.f})); PUT(9, p8); PUT(10, g1); PUT(11, cmul(p8, g1)); PUT(12, g2); PUT(13, cmul(p8, g2)); PUT(14, g3); PUT(15, cmul(p8, g3));
#pragma unroll
    for (int s = 0; s < 4; ++s) B[s] = *(LAS v4u*)(fr + s * 64);
    FS(1, 9); CFENCE(); LDW(w1, L_W1, 5); SB();
    {
      f2 z = cmul(g2, g2);
#pragma unroll
      for (int i = 0; i < 4; ++i) { PUT(2 * i, z); PUT(2 * i + 1, cmul(z, p8)); z = cmul(z, g1); }
      PUT(8, z);
    }
    kp[1] = f4{0, 0, 0, 0};
#pragma unroll
    for (int s = 0; s < 4; ++s) kp[1] = mfma(A[s], B[s], kp[1]);
    SB(); LDW(w1, L_W1, 6); SB();
#pragma unroll
    for (int s = 0; s < 4; ++s) B[s] = *(LAS v4u*)(fr + s * 64);
    kp[2] = f4{0, 0, 0, 0};
#pragma unroll
    for (int s = 0; s < 4; ++s) kp[2] = mfma(A[s], B[s], kp[2]);
    CFENCE();
#undef PUT
  }
  FS(1, 10);
  STAMP(2);

  v4u xnp;
  {
    asm volatile("s_waitcnt vmcnt(5)" : "+v"(X[0]), "+v"(X[1]), "+v"(X[2]), "+v"(X[3]), "+v"(X[4]), "+v"(X[5]), "+v"(X[6])::"memory");
    STAMP(3); FS(1, 11);
    f4 G = {0, 0, 0, 0}, Sx = {0, 0, 0, 0};
#pragma unroll
    for (int b = 0; b < 7; ++b) { G = mfma(X[b], X[b], G); Sx = mfma(ones, X[b], Sx); }
    const float gd = (c & 2) ? ((c & 1) ? G[3] : G[2]) : ((c & 1) ? G[1] : G[0]);
    if (q == (c >> 2)) *(LAS f2*)(S + L_ST + (ti * 16 + c) * 16 + u * 8) = f2{Sx[0], gd};
    FS(1, 12);
    LDW(w2, L_W2, 1); LDW(w2, L_W2, 2);
    VMWAIT(2);
    FS(1, 13);
    BAR();
    FS(1, 14);
    STAMP(4);
    f4 acc[4] = {{0, 0, 0, 0}, {0, 0, 0, 0}, {0, 0, 0, 0}, {0, 0, 0, 0}}, cs = {0, 0, 0, 0};
    const bool hi2 = (ti & 2) != 0;
    v4u A[2][4];
#pragma unroll
    for (int p = 0; p < 4; ++p) A[0][p] = trfrag(S + L_W1 + wa[p], 0, 512);
#pragma unroll
    for (int b = 0; b < 7; ++b) {
      if (b < 6) {
#pragma unroll
        for (int p = 0; p < 4; ++p) A[(b + 1) & 1][p] = trfrag(S + L_W1 + wa[p], (b + 1) * 8192, (b + 1) * 8192 + 512);
      }
      if (b < 4) LDW(w2, L_W2, 3 + b);
      SB();
#pragma unroll
      for (int p = 0; p < 4; ++p) acc[p] = mfma(A[b & 1][p], X[b], acc[p]);
      { const v4u s0 = A[b & 1][0], s2 = A[b & 1][2];
        cs = mfma(v4u{hi2 ? s2.x : s0.x, hi2 ? s2.y : s0.y, hi2 ? s2.z : s0.z, hi2 ? s2.w : s0.w}, ones, cs); }
      SB();
      FS(1, 15 + b);
    }
    *(LAS f4*)(S + L_C + w * FBS + lane * 16) = acc[2];
    *(LAS f4*)(S + L_C + w * FBS + 1024 + lane * 16) = acc[3];
    if (c == 0) *(LAS f4*)(S + L_CS + u * 256 + ((ti & 2) ^ (2 * u) ^ (ti & 1)) * 64 + q * 16) = cs;
    FS(1, 22);
    BAR();
    FS(1, 23);
    STAMP(5);
    const f4 st = *(LAS f4*)(S + L_ST + (ti * 16 + c) * 16);
    const f4 o0 = *(LAS f4*)(S + L_C + (w ^ 1) * FBS + lane * 16), o1 = *(LAS f4*)(S + L_C + (w ^ 1) * FBS + 1024 + lane * 16);
    const int t0 = 2 * u + (ti & 1);
    const f4 c0 = *(LAS f4*)(S + L_CS + t0 * 64 + q * 16) + *(LAS f4*)(S + L_CS + 256 + t0 * 64 + q * 16);
    const f4 c1 = *(LAS f4*)(S + L_CS + (t0 ^ 1) * 64 + q * 16) + *(LAS f4*)(S + L_CS + 256 + (t0 ^ 1) * 64 + q * 16);
    const float mean = (st[0] + st[2]) * (1.f / 448.f);
    const float var = __builtin_fmaxf((st[1] + st[3]) * (1.f / 448.f) - mean * mean, 0.f);
    const float r1 = __builtin_amdgcn_rsqf(var + 1e-5f);
    const float rs2 = r1 * __builtin_amdgcn_rsqf(var * r1 * r1 + 1e-5f), nm = -mean * rs2;
    float h[8];
#pragma unroll
    for (int r = 0; r < 4; ++r) {
      h[r] = gelu(__builtin_fmaf(acc[0][r] + o0[r], rs2, nm * c0[r]));
      h[4 + r] = gelu(__builtin_fmaf(acc[1][r] + o1[r], rs2, nm * c1[r]));
    }
    const u32 a0 = pk(h[0], h[1]), a1 = pk(h[2], h[3]), b0 = pk(h[4], h[5]), b1 = pk(h[6], h[7]);
    v4u hv = {sg ? b0 : a0, sg ? b1 : a1, sg ? a0 : b0, sg ? a1 : b1};
    if ((int)hoff < 0) hv = v4u{0, 0, 0, 0};
    __builtin_amdgcn_raw_buffer_store_b128(hv, rs, hoff & 0x7fffffffu, O_HS + (2 * j + u) * 1024, AUX_SC1);
    STAMP(6); FS(1, 24);
    const v4u xpc = X[0];
    *(LAS v4u*)(S + L_C + w * FBS + 3072 + lane * 16) = xpc;
    const float m1 = -mean * r1;
#pragma unroll
    for (int k = 0; k < 4; ++k) xnp[k] = pk(__builtin_fmaf(blo(xpc[k]), r1, m1), __builtin_fmaf(bhi(xpc[k]), r1, m1));
  }

  v4u W3[28];
#define LDW3(k) W3[k] = __builtin_amdgcn_raw_buffer_load_b128(rs, lane * 16 + ((k) & 3) * 1024, O_W3T + (8 * j + w) * 28672 + ((k) >> 2) * 4096, AUX_SC1)
  {
    LAS char* xt = S + L_XT + (16 * q + 8 * u) * 144 + (16 * ti + c) * 2;
#pragma unroll
    for (int k = 0; k < 4; ++k) {
      *(LAS u16*)(xt + (2 * k) * 144) = (u16)xnp[k];
      *(LAS u16*)(xt + (2 * k + 1) * 144) = (u16)(xnp[k] >> 16);
    }
    BAR();
    const v4u tv = *(LAS v4u*)(S + L_XT + (8 * w + (lane >> 3)) * 144 + (lane & 7) * 16);
#pragma unroll
    for (int k = 0; k < 3; ++k) *(LAS f4*)(S + L_C + w * FBS + (16 * k + c) * 64 + q * 16) = kp[k];
    if (tid < 32) *(LAS v4u*)(S + L_KC + (tid >> 2) * KCS + 4096 + (tid & 3) * 16) = v4u{0, 0, 0, 0};
    else if (tid < 160) *(LAS v4u*)(S + L_U + ((tid - 32) >> 4) * 1280 + (tid & 15) * 16) = v4u{0, 0, 0, 0};
    else if (tid < 162) *(LAS v2u*)(S + L_KT + (tid - 160) * KTS) = v2u{0, 0};
    VMWAIT(0);
    if (lane == 0) set_flag(flags + F_HF + (u * 128 + T) * 8 + j);
    STAMP(7);
    __builtin_amdgcn_raw_buffer_store_b128(tv, rs, (64 * j + 8 * w + (lane >> 3)) * 4096 + (lane & 7) * 16, O_XNT + cl * 128, AUX_SC1);
    BAR();
    const int tp = tid & 255;
    if (tp < 164) {
      LAS char* pp4 = S + L_C + ch * 4 * FBS + tp * 16;
      const f4 v = (*(LAS f4*)(pp4) + *(LAS f4*)(pp4 + FBS)) + (*(LAS f4*)(pp4 + 2 * FBS) + *(LAS f4*)(pp4 + 3 * FBS));
      const int col = tp >> 2;
      int cv;
      if (col < 8) cv = 2 * col + 1;
      else if (col < 16) cv = 2 + 4 * (col & 3) + 256 * ((col - 8) >> 2);
      else if (col < 24) cv = 4 + 8 * (col & 1) + 256 * ((col - 16) >> 1);
      else cv = 8 * (col & 1) + 256 * ((col - 24) >> 1);
      LAS char* kt = S + L_KT + ch * KTS + 8 + cv * 2 + (tp & 3) * 128;
      const u32 k01 = pk(v[0], v[1]), k23 = pk(v[2], v[3]);
      if (tp <= 160) *(LAS u16*)(kt) = (u16)k01;
      if (tp < 160) { *(LAS u16*)(kt + 32) = (u16)(k01 >> 16); *(LAS u16*)(kt + 64) = (u16)k23; *(LAS u16*)(kt + 96) = (u16)(k23 >> 16); }
    }
    BAR();
    {
      const float m0 = (float)(2047 - 8 * tp);
      u32 v[12];
#pragma unroll
      for (int e = 0; e < 12; e += 2) {
        const u32 r = pk(m0 - (float)e, m0 - (float)(e + 1));
        v[e] = *(LAS u16*)(S + L_KT + ch * KTS + 8 + 2 * (int)blo(r));
        if (e < 10) v[e + 1] = *(LAS u16*)(S + L_KT + ch * KTS + 8 + 2 * (int)bhi(r));
      }
      u32 pr[10];
#pragma unroll
      for (int e = 0; e < 10; ++e) pr[e] = v[e] | (v[e + 1] << 16);
#pragma unroll
      for (int k = 0; k < 4; ++k) *(LAS v4u*)(S + L_KC + (ch * 4 + k) * KCS + tp * 16) = v4u{pr[3 - k], pr[5 - k], pr[7 - k], pr[9 - k]};
    }
    VMWAIT(0);
    BAR();
    if (tid == 0) set_flag(flags + F_XT + j * 32 + cl);
    STAMP(8);
  }

  f4 cs3a, cs3b;
  {
    v4u A[28];
#pragma unroll
    for (int b = 0; b < 7; ++b) {
#pragma unroll
      for (int p = 0; p < 4; ++p) A[4 * b + p] = trfrag(S + L_W2 + wa[p], b * 8192, b * 8192 + 512);
      SB();
    }
    {
      const int jj = lane & 7, dt_ = lane >> 3;
      wait_flags(dt_ < 3 ? flags + F_HF + (u * 128 + ((T + dt_ - 1) & 127)) * 8 + (jj < 7 ? jj : 6) : flags + F_W3 + 4 * j + ti, true);
    }
    STAMP(9);
    v4u H[7];
#pragma unroll
    for (int b = 0; b < 7; ++b) H[b] = __builtin_amdgcn_raw_buffer_load_b128(rs, lane * 16, O_HS + (T * 14 + 2 * blkof[b] + u) * 1024, AUX_SC1);
    SB();
    f4 acc[4] = {{0, 0, 0, 0}, {0, 0, 0, 0}, {0, 0, 0, 0}, {0, 0, 0, 0}};
#pragma unroll
    for (int b = 0; b < 7; ++b) {
#pragma unroll
      for (int p = 0; p < 4; ++p) acc[p] = mfma(A[4 * b + p], H[b], acc[p]);
      LDW3(b);
      SB();
    }
    *(LAS f4*)(S + L_C + w * FBS + lane * 16) = acc[2];
    *(LAS f4*)(S + L_C + w * FBS + 1024 + lane * 16) = acc[3];
    BAR();
    STAMP(10);
    const f4 o0 = *(LAS f4*)(S + L_C + (w ^ 1) * FBS + lane * 16), o1 = *(LAS f4*)(S + L_C + (w ^ 1) * FBS + 1024 + lane * 16);
    const u32 n0 = sg ? xnp[2] : xnp[0], n1 = sg ? xnp[3] : xnp[1], n2 = sg ? xnp[0] : xnp[2], n3 = sg ? xnp[1] : xnp[3];
    const u32 a0 = pk(acc[0][0] + o0[0] + blo(n0), acc[0][1] + o0[1] + bhi(n0)), a1 = pk(acc[0][2] + o0[2] + blo(n1), acc[0][3] + o0[3] + bhi(n1));
    const u32 b0 = pk(acc[1][0] + o1[0] + blo(n2), acc[1][1] + o1[1] + bhi(n2)), b1 = pk(acc[1][2] + o1[2] + blo(n3), acc[1][3] + o1[3] + bhi(n3));
    const v4u x1p = v4u{sg ? b0 : a0, sg ? b1 : a1, sg ? a0 : b0, sg ? a1 : b1};
    __builtin_amdgcn_raw_buffer_store_b128(x1p, rs, lane * 16, O_X1 + (T * 14 + 2 * j + u) * 1024, AUX_SC1);
    *(LAS v4u*)(S + L_C + w * FBS + 2048 + lane * 16) = x1p;
    float s1 = 0.f, s2 = 0.f;
#pragma unroll
    for (int k = 0; k < 4; ++k) {
      const float a = blo(x1p[k]), b = bhi(x1p[k]);
      s1 += a + b; s2 = __builtin_fmaf(a, a, __builtin_fmaf(b, b, s2));
    }
    s1 += __shfl_xor(s1, 16); s2 += __shfl_xor(s2, 16);
    s1 += __shfl_xor(s1, 32); s2 += __shfl_xor(s2, 32);
    if (q == 0) __builtin_amdgcn_raw_buffer_store_b64(v2u{__builtin_bit_cast(u32, s1), __builtin_bit_cast(u32, s2)}, rs, c * 8, O_ST + ((T * 2 + u) * 7 + j) * 128, AUX_SC1);
    STAMP(11);
  }

  {
    wait_flags(flags + F_XT + j * 32 + (lane & 31), true);
    VMWAIT(0);
    if (lane == 0) set_flag(flags + F_X1 + (T * 2 + u) * 8 + j);
    STAMP(12);
    const int tp = tid & 255;
    const v4u uv = __builtin_amdgcn_raw_buffer_load_b128(rs, tp * 16, O_XNT + dch * 4096, AUX_SC1);
    SB();
    LDW3(7); LDW3(8);
    cs3a = __builtin_bit_cast(f4, __builtin_amdgcn_raw_buffer_load_b128(rs, q * 32, O_CS3 + (8 * j + w) * 128, AUX_SC1));
    cs3b = __builtin_bit_cast(f4, __builtin_amdgcn_raw_buffer_load_b128(rs, q * 32 + 16, O_CS3 + (8 * j + w) * 128, AUX_SC1));
    SB();
    *(LAS v4u*)(S + L_U + ch * 5120 + (tp & 3) * 1280 + 256 + (tp >> 2) * 16) = uv;
    BAR();
    STAMP(13);
    const int rho = (w >> 1) & 1;
    v2u sa, sb;
    LAS char* pa_ = S + L_KC + (ch * 4 + (c & 3)) * KCS + 2 * (2044 - (c & 12) + 8 * q - 16 * rho - 32 * u) - 3072 - 896;
    LAS char* pb_ = S + L_U + ch * 5120 + q * 1280 + (16 + c - u) * 16 - 224;
    f4 y[4] = {{0, 0, 0, 0}, {0, 0, 0, 0}, {0, 0, 0, 0}, {0, 0, 0, 0}};
    v4u B[2][4], A[2][4];
#define CONVLD(it) _Pragma("unroll") for (int a = 0; a < 4; ++a) { \
      B[(it) & 1][a] = *(LAS v4u*)(pb_ + (7 - (it)) * 32 + a * 256); \
      const v2u lo = *(volatile LAS v2u*)(pa_ + (7 - (it)) * 128 + (3 - a) * 1024), hi = *(volatile LAS v2u*)(pa_ + (7 - (it)) * 128 + (3 - a) * 1024 + 8); \
      A[(it) & 1][a] = v4u{lo.x, lo.y, hi.x, hi.y}; }
    CONVLD(0);
#pragma unroll
    for (int it = 0; it < 8; ++it) {
      if (it < 7) { CONVLD(it + 1); }
      LDW3(9 + 2 * it); LDW3(10 + 2 * it);
      SB();
#pragma unroll
      for (int a = 0; a < 4; ++a)
#pragma unroll
        for (int k = a; k < 4; ++k) y[k] = mfma(A[it & 1][a], B[it & 1][k - a], y[k]);
      SB();
      if (it == XPOLL_IT) {
        wait_flags(lane < 8 ? flags + F_X1 + (T * 2 + u) * 8 + (lane < 7 ? lane : 6) : flags + F_W4 + 4 * ((lane & 7) < 7 ? (lane & 7) : 6) + ti, true);
        STAMP(15);
#pragma unroll
        for (int b = 0; b < 7; ++b) dma_sc1(sbase + L_W2 + (ti * 14 + 2 * b + u) * 1024, lane * 16, ws + O_X1 + (T * 14 + 2 * b + u) * 1024);
        sa = __builtin_amdgcn_raw_buffer_load_b64(rs, q * 128 + c * 8, O_ST + (T * 2 + u) * 896, AUX_SC1);
        sb = __builtin_amdgcn_raw_buffer_load_b64(rs, (q < 3 ? q + 4 : q) * 128 + c * 8, O_ST + (T * 2 + u) * 896, AUX_SC1);
        SB();
      }
    }
    LDW3(25); LDW3(26); LDW3(27);
    STAMP(14);
    SB();
    const f4 e0 = u ? y[0] : y[2], e1 = u ? y[1] : y[3], k0 = u ? y[2] : y[0], k1 = u ? y[3] : y[1];
    *(LAS f4*)(S + L_C + w * FBS + lane * 16) = e0;
    *(LAS f4*)(S + L_C + w * FBS + 1024 + lane * 16) = e1;
    BAR();
#pragma unroll
    for (int k = 0; k < 2; ++k) {
      const f4 v = (k ? k1 : k0) + *(LAS f4*)(S + L_C + (w ^ 1) * FBS + k * 1024 + lane * 16);
      const int blk = 16 * (2 * u + k) + c;
      const v2u uu = *(LAS v2u*)(S + L_U + ch * 5120 + (2 * rho + (q >> 1)) * 1280 + (16 + blk) * 16 + (q & 1) * 8);
      const v2u o = {pk(__builtin_fmaf(blo(uu.x), dskip, v[0]), __builtin_fmaf(bhi(uu.x), dskip, v[1])),
                     pk(__builtin_fmaf(blo(uu.y), dskip, v[2]), __builtin_fmaf(bhi(uu.y), dskip, v[3]))};
      *(LAS v2u*)(S + L_KT + ch * 4096 + blk * 64 + rho * 32 + q * 8) = o;
    }
    {
      float s1 = asf(sa.x) + (q < 3 ? asf(sb.x) : 0.f);
      float s2 = asf(sa.y) + (q < 3 ? asf(sb.y) : 0.f);
      s1 += __shfl_xor(s1, 16); s2 += __shfl_xor(s2, 16);
      s1 += __shfl_xor(s1, 32); s2 += __shfl_xor(s2, 32);
      if (q == 0) *(LAS f2*)(S + L_ST + (ti * 16 + c) * 16 + u * 8) = f2{s1, s2};
    }
    VMWAIT(0);
    BAR();
    const v4u sv = *(LAS v4u*)(S + L_KT + tid * 16);
    __builtin_amdgcn_raw_buffer_store_b128(sv, rs, tp * 16, O_ST2 + dch * 4096, AUX_SC1);
    STAMP(16);
  }

  v4u W4[28], own[4], H2[4][6];
  const u32 w4b = O_W4T + (j * 8 + w) * 28672;
#define LDW4(k) W4[k] = __builtin_amdgcn_raw_buffer_load_b128(rs, lane * 16 + ((k) & 3) * 1024, w4b + blkof[(k) >> 2] * 4096, AUX_SC1)
#define LDH2(s) _Pragma("unroll") for (int b = 1; b < 7; ++b) \
    H2[s][b - 1] = __builtin_amdgcn_raw_buffer_load_b128(rs, lane * 16, O_H2 + ((cl * 4 + ((ti + 1 + (s)) & 3)) * 56 + blkof[b] * 8 + w) * 1024, AUX_SC1)
#define WAITH2(s) wait_flags(flags + F_H2 + ((cl * 4 + ((ti + 1 + (s)) & 3)) * 8 + w) * 8 + (lane & 7), (lane & 7) < 7 && (lane & 7) != j)
  if (w < 4) __builtin_amdgcn_s_setprio(1);
  v4u Bq[4];
#pragma unroll
  for (int k = 0; k < 3; ++k) Bq[k] = *(LAS v4u*)(S + L_W2 + ((ti + 1) & 3) * 14336 + lane * 16 + k * 1024);
#pragma unroll
  for (int s = 0; s < 4; ++s) {
    const int tl = (ti + 1 + s) & 3;
    LAS char* ap = S + L_W2 + tl * 14336 + lane * 16;
    LAS char* an = S + L_W2 + ((tl + 1) & 3) * 14336 + lane * 16;
    f4 a0 = {0, 0, 0, 0}, a1 = {0, 0, 0, 0};
#pragma unroll
    for (int ks = 0; ks < 14; ++ks) {
      const int qi = (s * 14 + ks) & 3, qn = (s * 14 + ks + 3) & 3;
      if (ks + 3 < 14) Bq[qn] = *(LAS v4u*)(ap + (ks + 3) * 1024);
      else if (s < 3) Bq[qn] = *(LAS v4u*)(an + (ks - 11) * 1024);
      if (s < 3 && ks % 3 == 0 && s * 5 + ks / 3 < 14) LDW4(s * 5 + ks / 3);
      SB();
      a0 = mfma(W3[2 * ks], Bq[qi], a0);
      a1 = mfma(W3[2 * ks + 1], Bq[qi], a1);
      if (s == 3) LDW4(14 + ks);
      SB();
    }
    if (s == 0) {
      VMWAIT(5);
      BAR();
      if (tid == 0) set_flag(flags + F_S + j * 32 + cl);
    } else {
      if (s == 1) VMWAIT(5);
      if (s == 2) VMWAIT(4);
      if (s == 3) VMWAIT(20);
      if (lane == 0) set_flag(flags + F_H2 + ((cl * 4 + ((ti + s) & 3)) * 8 + w) * 8 + j);
    }
    const f4 st = *(LAS f4*)(S + L_ST + (tl * 16 + c) * 16);
    const float mean = (st[0] + st[2]) * (1.f / 448.f);
    const float rstd = __builtin_amdgcn_rsqf(__builtin_fmaxf((st[1] + st[3]) * (1.f / 448.f) - mean * mean, 0.f) + 1e-5f), nm = -mean * rstd;
    float h[8];
#pragma unroll
    for (int r = 0; r < 4; ++r) {
      h[r] = gelu(__builtin_fmaf(a0[r], rstd, nm * cs3a[r]));
      h[4 + r] = gelu(__builtin_fmaf(a1[r], rstd, nm * cs3b[r]));
    }
    own[s] = v4u{pk(h[0], h[1]), pk(h[2], h[3]), pk(h[4], h[5]), pk(h[6], h[7])};
    __builtin_amdgcn_raw_buffer_store_b128(own[s], rs, lane * 16, O_H2 + ((cl * 4 + tl) * 56 + j * 8 + w) * 1024, AUX_SC1);
    SB();
    if (s == 2) { WAITH2(0); LDH2(0); SB(); }
    STAMP(17 + s);
  }
  f4 o[4][4];
#pragma unroll
  for (int s = 0; s < 4; ++s)
#pragma unroll
    for (int t = 0; t < 4; ++t) o[s][t] = mfma(W4[t], own[s], f4{0, 0, 0, 0});
  SB();
  WAITH2(1);
  VMWAIT(0);
  if (lane == 0) set_flag(flags + F_H2 + ((cl * 4 + ti) * 8 + w) * 8 + j);
  LDH2(1);
  SB();
  STAMP(21);
  f4 pre0 = {0, 0, 0, 0}, pre1 = {0, 0, 0, 0};
#pragma unroll
  for (int s = 0; s < 4; ++s) {
    const int tl = (ti + 1 + s) & 3;
    if (s == 3) {
#pragma unroll
      for (int k = 0; k < 6; ++k) {
        pre0 += *(LAS f4*)(S + (ti * 7 + k) * 4096 + (2 * u) * 1024 + lane * 16);
        pre1 += *(LAS f4*)(S + (ti * 7 + k) * 4096 + (2 * u + 1) * 1024 + lane * 16);
      }
    }
#pragma unroll
    for (int b = 1; b < 7; ++b)
#pragma unroll
      for (int t = 0; t < 4; ++t) o[s][t] = mfma(W4[4 * b + t], H2[s][b - 1], o[s][t]);
    SB();
    if (s < 2) { WAITH2(s + 2); LDH2(s + 2); SB(); }
    if (s == 0) BAR();
    if (s < 3) {
#pragma unroll
      for (int t = 0; t < 4; ++t) *(LAS f4*)(S + (tl * 7 + 2 * s + u) * 4096 + t * 1024 + lane * 16) = o[s][t];
    }
    if (s == 1) {
      wait_flags(flags + F_S + j * 32 + (lane & 31), true);
      dma_sc1(sbase + L_XT + w * 1024, (lane >> 3) * 4096 + (lane & 7) * 16, ws + O_ST2 + (64 * j + 8 * w) * 4096 + cl * 128);
    }
    if (s == 2) { VMWAIT(0); BAR(); }
    STAMP(22 + s);
  }
  {
    const f4 e0 = u ? o[3][0] : o[3][2], e1 = u ? o[3][1] : o[3][3], k0 = u ? o[3][2] : o[3][0], k1 = u ? o[3][3] : o[3][1];
    *(LAS f4*)(S + (ti * 7 + 6) * 4096 + (2 * (u ^ 1)) * 1024 + lane * 16) = e0;
    *(LAS f4*)(S + (ti * 7 + 6) * 4096 + (2 * (u ^ 1) + 1) * 1024 + lane * 16) = e1;
    const v4u xpc = *(LAS v4u*)(S + L_C + w * FBS + 3072 + lane * 16), x1p = *(LAS v4u*)(S + L_C + w * FBS + 2048 + lane * 16);
    const v4u sp_ = trfrag(S + L_XT + (16 * q + 8 * u + pa) * 128 + (16 * ti + 4 * pp) * 2, 0, 512);
    BAR();
    const f4 v0 = k0 + pre0 + *(LAS f4*)(S + (ti * 7 + 6) * 4096 + (2 * u) * 1024 + lane * 16);
    const f4 v1 = k1 + pre1 + *(LAS f4*)(S + (ti * 7 + 6) * 4096 + (2 * u + 1) * 1024 + lane * 16);
    float r[8];
#pragma unroll
    for (int k = 0; k < 8; ++k) {
      const float v = (k < 4 ? v0[k & 3] : v1[k & 3]) + ((k & 1) ? bhi(x1p[k >> 1]) : blo(x1p[k >> 1]));
      const float g = v * __builtin_amdgcn_rcpf(1.0f + __builtin_amdgcn_exp2f(v * -1.4426950409f));
      r[k] = __builtin_fmaf((k & 1) ? bhi(sp_[k >> 1]) : blo(sp_[k >> 1]), g, (k & 1) ? bhi(xpc[k >> 1]) : blo(xpc[k >> 1]));
    }
    *(GAS v4u*)((GAS char*)out + tok * 896 + 128 * j + 32 * q + 16 * u) = v4u{pk(r[0], r[1]), pk(r[2], r[3]), pk(r[4], r[5]), pk(r[6], r[7])};
    STAMP(26);
    while (__builtin_amdgcn_s_memrealtime() < tstart_ + 6000 + 4 * (tev_ - tstart_)) __builtin_amdgcn_s_sleep(2);
    DUMP();
  }
}

extern "C" void kernel_launch(void* const* d_in, const int* in_sizes, int n_in, void* d_out, int out_size, void* d_ws, size_t ws_size,
                              hipStream_t stream) {
  (void)hipMemsetAsync(d_ws, 0, FLAG_BYTES, stream);
  fused_kernel<<<256, 512, 0, stream>>>((const u16*)d_in[0], (const u16*)d_in[1], (const u16*)d_in[2], (const u16*)d_in[3], (const u16*)d_in[4],
                                        (const u16*)d_in[5], (const u16*)d_in[6], (const u16*)d_in[7], (const u16*)d_in[8], (const u16*)d_in[10],
                                        (const u16*)d_in[11], (const u16*)d_in[12], (const u16*)d_in[13], (u16*)d_out, (unsigned char*)d_ws);
}
```

```cpp
#include <hip/hip_runtime.h>
#include <type_traits>
typedef unsigned short u16;
typedef unsigned u32;
typedef unsigned long long u64;
typedef u32 v4u __attribute__((ext_vector_type(4)));
typedef u32 v2u __attribute__((ext_vector_type(2)));
typedef float f4 __attribute__((ext_vector_type(4)));
typedef float f2 __attribute__((ext_vector_type(2)));
typedef short s8v __attribute__((ext_vector_type(8)));
typedef short s4v __attribute__((ext_vector_type(4)));
typedef __bf16 bf2 __attribute__((ext_vector_type(2)));
#define LAS __attribute__((address_space(3)))
#define GAS __attribute__((address_space(1)))
#define CAS __attribute__((address_space(4)))
#define RLX_AGENT __ATOMIC_RELAXED, __HIP_MEMORY_SCOPE_AGENT
#define DEV __device__ __forceinline__
#define SB() __builtin_amdgcn_sched_barrier(0)
#define CFENCE() asm volatile("" ::: "memory")
#define VMWAIT(n) asm volatile("s_waitcnt vmcnt(" #n ")" ::: "memory")
#define BAR() asm volatile("s_waitcnt lgkmcnt(0)\n\ts_barrier" ::: "memory")
#define NOUNROLL _Pragma("clang loop unroll(disable)")

constexpr u32 F_S = 0, F_W3 = 1792, F_W4 = 1824, F_CLR = 1856, F_ACK = 2112;
constexpr u32 O_NON = 32768, O_ECH = 65536;
constexpr u32 O_HS = 131072, O_X1 = O_HS + 1835008, O_ST = O_X1 + 1835008, O_H2 = O_ST + 229376, O_XNT = O_H2 + 7340032,
              O_ST2 = O_XNT + 1835008, O_W3T = O_ST2 + 1835008, O_W4T = O_W3T + 1605632, O_CS3 = O_W4T + 1605632,
              O_DBG = O_CS3 + 8192, WS_END = O_DBG + (1 << 20);
constexpr u32 L_W1 = 0, L_W2 = 57344, L_C = 114688, FBS = 4608, L_XT = L_C + 8 * FBS, L_ST = L_XT + 9216, L_CS = L_ST + 1024;
constexpr u32 L_KC = 0, KCS = 4160, L_U = 33280, L_KT = 43520, KTS = 4128;
constexpr int AUX_SC1 = 16, SPIN = 1 << 15;
constexpr u32 SENT = 0xffffffffu;
#define XIT 1

#define SPM 0
#define STAMP(k)
#define CSTAMP(k)
#define DUMP()
#define RETRY(k)

DEV f4 mfma(v4u a, v4u b, f4 c) { return __builtin_amdgcn_mfma_f32_16x16x32_bf16(__builtin_bit_cast(s8v, a), __builtin_bit_cast(s8v, b), c, 0, 0, 0); }
DEV u32 pk2(f2 a) { return __builtin_bit_cast(u32, __builtin_convertvector(a, bf2)); }
DEV u32 pk(float a, float b) { return pk2(f2{a, b}); }
DEV float asf(u32 v) { return __builtin_bit_cast(float, v); }
DEV u32 asu(float v) { return __builtin_bit_cast(u32, v); }
DEV float blo(u32 v) { return asf(v << 16); }
DEV float bhi(u32 v) { return asf(v & 0xffff0000u); }
DEV f2 unpk(u32 v) { return f2{blo(v), bhi(v)}; }
DEV f2 sp2(float v) { return f2{v, v}; }
DEV f2 fma2(f2 a, f2 b, f2 c) { return __builtin_elementwise_fma(a, b, c); }
DEV f2 sigm2(f2 t) {
  const f2 d = f2{__builtin_amdgcn_exp2f(t.x), __builtin_amdgcn_exp2f(t.y)} + sp2(1.0f);
  return f2{__builtin_amdgcn_rcpf(d.x), __builtin_amdgcn_rcpf(d.y)};
}
DEV u32 gelu_pk(f2 z) { return pk2(z * sigm2(z * fma2(z * z, sp2(-0.1029432397f), sp2(-2.302208198f)))); }
DEV v2u trd(LAS char* p) { return __builtin_bit_cast(v2u, __builtin_amdgcn_ds_read_tr16_b64_v4i16((LAS s4v*)p)); }
DEV v4u trfrag(LAS char* p, int o0, int o1) { const v2u a = trd(p + o0), b = trd(p + o1); return v4u{a.x, a.y, b.x, b.y}; }
DEV v4u mkdesc(const void* p) { const u64 a = (u64)p; return v4u{(u32)a, (u32)(a >> 32) & 0xffffu, 0x7fffffffu, 0x20000u}; }
DEV void dma(u32 la, u32 lb, u32 voff, v4u d, u32 soff) {
  asm volatile("s_add_u32 m0, %0, %1\n\ts_nop 0\n\tbuffer_load_dwordx4 %2, %3, %4 offen lds" ::"s"(la), "s"(lb), "v"(voff), "s"(d), "s"(soff) : "scc");
}
template <int LB> DEV void dmai(u32 la, u32 voff, v4u d, u32 soff) {
  asm volatile("s_add_u32 m0, %0, %1\n\ts_nop 0\n\tbuffer_load_dwordx4 %2, %3, %4 offen lds" ::"s"(la), "i"(LB), "v"(voff), "s"(d), "s"(soff) : "scc");
}
template <int LB> DEV void dmai_sc1(u32 la, u32 voff, v4u d, u32 soff) {
  asm volatile("s_add_u32 m0, %0, %1\n\ts_nop 0\n\tbuffer_load_dwordx4 %2, %3, %4 offen sc1 lds" ::"s"(la), "i"(LB), "v"(voff), "s"(d), "s"(soff) : "scc");
}
template <int N, class F> DEV void unroll(F f) { if constexpr (N > 0) { unroll<N - 1>(f); f(std::integral_constant<int, N - 1>{}); } }
DEV u32 ldflag(GAS u32* p) { return __hip_atomic_load(p, RLX_AGENT); }
DEV void stflag(GAS u32* p, u32 v) { __hip_atomic_store(p, v, RLX_AGENT); }
DEV void wait_flags(GAS u32* p, u32 v) {
NOUNROLL
  for (int spin = 0; spin < SPIN && !__all(v != 0u); ++spin) v = ldflag(p);
  __builtin_amdgcn_fence(__ATOMIC_ACQUIRE, "wavefront");
}
DEV u32 max3u(u32 a, u32 b, u32 c) { return max(max(a, b), c); }
DEV f2 cmul(f2 a, f2 b) {
  f2 r;
  asm("v_pk_mul_f32 %0, %1, %2 op_sel:[0,0] op_sel_hi:[0,1]\n\tv_pk_fma_f32 %0, %1, %2, %0 op_sel:[1,1,0] op_sel_hi:[1,0,1] neg_lo:[0,1,0]"
      : "=&v"(r) : "v"(a), "v"(b));
  return r;
}
DEV f2 cexp2(float er, float ph) {
  const float f = __builtin_amdgcn_fractf(ph);
  return sp2(__builtin_amdgcn_exp2f(er)) * f2{__builtin_amdgcn_cosf(f), __builtin_amdgcn_sinf(f)};
}
DEV int nxt7(int b8) { return b8 == 6 * 8192 ? 0 : b8 + 8192; }

__global__ void __launch_bounds__(512, 2)
fused_kernel(const u16*, const u16*, const u16*, const u16*, const u16*, const u16*, const u16*, const u16*, const u16*, const u16*,
             const u16*, const u16*, const u16*, u16*, unsigned char*) {
  __shared__ __attribute__((aligned(16))) char smem[163840];
  LAS char* const S = (LAS char*)smem;
  const u32 sb0 = (u32)(size_t)S;
  const int tid = threadIdx.x, lane = tid & 63, w = __builtin_amdgcn_readfirstlane(tid >> 6);
  typedef u64 u64x8 __attribute__((ext_vector_type(8)));
  u64x8 ka;
  u64 px, parr;
  asm volatile("s_load_dwordx16 %0, %3, 0x38\n\ts_load_dwordx2 %1, %3, 0x0\n\ts_load_dwordx2 %2, %3, %4 offset:0x8"
               : "=&s"(ka), "=&s"(px), "=&s"(parr) : "s"(__builtin_amdgcn_kernarg_segment_ptr()), "s"(min(w, 5) * 8));
  const int c = lane & 15, q = lane >> 4, pa = (lane >> 2) & 3, pp = lane & 3;
  const int xcd = blockIdx.x & 7, rk = blockIdx.x >> 3;
  int cq = ((rk - 4) * 37) >> 8, j = (rk - 4) - 7 * cq, cl = xcd * 4 + cq;
  u32 wk = sb0 + w * 1024;
  const u32 rowo = (8 * w + (lane >> 3)) * 896;
  u32 xo = rowo + 16 * ((lane & 7) ^ ((4 * w + q + 6) & 7)), wo = rowo + 16 * ((lane & 7) ^ ((q & 1) | (w & 2))), l16 = lane * 16;
  asm volatile("" : "+v"(xo), "+v"(wo), "+v"(l16));
  asm volatile("s_waitcnt lgkmcnt(0)" : "+s"(ka), "+s"(px), "+s"(parr), "+s"(wk), "+s"(cq), "+s"(j), "+s"(cl));
  const char *x = (const char*)px, *w1 = (const char*)ka[2], *w2 = (const char*)ka[3], *w3 = (const char*)ka[4], *w4 = (const char*)ka[5];
  const u16 *ldt = (const u16*)ka[0], *dsk = (const u16*)ka[1];
  u16* const out = (u16*)ka[6];
  unsigned char* const ws = (unsigned char*)ka[7];
  GAS u32* const flags = (GAS u32*)ws;
  GAS u32* const pnon = (GAS u32*)(ws + O_NON) + blockIdx.x * 32;
  GAS u32* const pech = (GAS u32*)(ws + O_ECH) + blockIdx.x * 32;
  const __amdgpu_buffer_rsrc_t rs = __builtin_amdgcn_make_buffer_rsrc(ws, 0, WS_END, 0x00020000);
  const v4u ones = {0x3f803f80u, 0x3f803f80u, 0x3f803f80u, 0x3f803f80u};
#define LDWS(vo, so) __builtin_amdgcn_raw_buffer_load_b128(rs, vo, so, AUX_SC1)
#define STWS(v, vo, so) do { const v4u sv_ = (v); __builtin_amdgcn_raw_buffer_store_b128(sv_, rs, vo, so, AUX_SC1); asm volatile("s_nop 1" ::"v"(sv_) : "memory"); } while (0)
  CSTAMP(30);
  STAMP(0);

  if (rk < 4) {
    const int sp = xcd * 4 + rk;
    STAMP(4 + SPM);
    if (sp >= 28) {
      if (sp > 28) return;
      STWS((v4u{0, 0, 0, 0}), tid * 16, 0);
      STWS((v4u{0, 0, 0, 0}), tid * 16, 8192);
      VMWAIT(0);
      BAR();
      if (w >= 4) return;
      const int b = w * 64 + lane;
      const bool idle = b < 32 && (b & 7) == 7;
      u32 last = 0;
NOUNROLL
      for (int spin = 0; spin < 4 * SPIN; ++spin) {
        const u32 v = ldflag((GAS u32*)(ws + O_NON) + b * 32), a = ldflag(flags + F_ACK + b);
        if (v != last || spin == 0) stflag((GAS u32*)(ws + O_ECH) + b * 32, v);
        last = v;
        if (__all(a != 0u || idle)) break;
      }
      return;
    }
    if (w == 7) {
      const u32 e = ldflag(pech), n = ldflag(pnon), non = (e + 1 == n) ? e + 2 : e + 1;
      if (lane == 0) stflag(pnon, non);
      BAR();
NOUNROLL
      for (int spin = 0; spin < SPIN && !__all(ldflag(pech) == non); ++spin) {}
      if (lane == 0) stflag(flags + F_ACK + blockIdx.x, 1u);
      BAR();
      return;
    }
    const u32 lb = sb0 + w * 16384;
    const v4u d3 = mkdesc((const char*)w3 + w * 64 * 3584 + sp * 128), d4 = mkdesc((const char*)w4 + sp * 64 * 896 + w * 128);
    const u32 v3 = (lane >> 3) * 3584 + (lane & 7) * 16, v4 = (lane >> 3) * 896 + (lane & 7) * 16;
    unroll<8>([&](auto g) { dmai<((2 * g & 7) + (g >> 2)) * 1024>(lb, v3, d3, ((2 * g & 7) + (g >> 2)) * 8 * 3584); });
    {
      LAS char* p = S + w * 16384 + (16 * q + pa) * 128 + 16 * pp;
      f4 cs[4] = {{0, 0, 0, 0}, {0, 0, 0, 0}, {0, 0, 0, 0}, {0, 0, 0, 0}};
#pragma unroll
      for (int u = 0; u < 2; ++u) {
        VMWAIT(4);
        if (u == 0) STAMP(1 + SPM);
        v4u a[4];
#pragma unroll
        for (int et = 0; et < 4; ++et) a[et] = trfrag(p, u * 1024 + 64 * (et >> 1) + 8 * (et & 1), u * 1024 + 64 * (et >> 1) + 8 * (et & 1) + 512);
#pragma unroll
        for (int et = 0; et < 4; ++et) {
          STWS(a[et], l16, O_W3T + (((2 * sp + (et >> 1)) * 14 + 2 * w + u) * 2 + (et & 1)) * 1024);
          cs[et] = mfma(a[et], ones, cs[et]);
        }
      }
#pragma unroll
      for (int et = 0; et < 4; ++et)
        if (c == 0) *(LAS f4*)(S + 114688 + w * 256 + (32 * (et >> 1) + 8 * q + 4 * (et & 1)) * 4) = cs[et];
    }
    BAR();
    if (w == 0) {
      float s = 0.f;
#pragma unroll
      for (int k = 0; k < 7; ++k) s += *(LAS float*)(S + 114688 + k * 256 + lane * 4);
      __builtin_amdgcn_raw_buffer_store_b32(asu(s), rs, lane * 4, O_CS3 + sp * 256, AUX_SC1);
    }
    VMWAIT(0);
    BAR();
    if (tid == 0) stflag(flags + F_W3 + sp, 1u);
    STAMP(2 + SPM);
    unroll<8>([&](auto g) { dmai<8192 + g * 1024>(lb, v4, d4, g * 8 * 896); });
    VMWAIT(0);
    {
      LAS char* p = S + w * 16384 + 8192 + (8 * q + pa) * 128 + 32 * pp;
      v4u a[8];
#pragma unroll
      for (int k = 0; k < 8; ++k) a[k] = trfrag(p, (k >> 2) * 4096 + 8 * (k & 3), (k >> 2) * 4096 + 512 + 8 * (k & 3));
#pragma unroll
      for (int k = 0; k < 8; ++k) {
        const int fb = 2 * sp + (k >> 2);
        STWS(a[k], l16, O_W4T + (((w * 8 + (fb & 7)) * 7 + (fb >> 3)) * 4 + (k & 3)) * 1024);
      }
    }
    VMWAIT(0);
    BAR();
    if (tid == 0) stflag(flags + F_W4 + sp, 1u);
    STAMP(3 + SPM);
    DUMP();
    return;
  }

  const int ti = w >> 1, u = w & 1, T = cl * 4 + ti;
  const int ch = w >> 2, dch = 64 * j + 2 * cl + ch;

  const v4u dx = mkdesc((const char*)x + cl * 57344), dw1 = mkdesc((const char*)w1 + j * 128), dw2 = mkdesc((const char*)w2 + j * 128);
  const u32 wk2 = wk + L_W2;
  int xb8 = j * 8192, wb8 = cq * 16384;
  u32 ee = 0, en = 0;
#define LDX() do { dma(wk2, xb8, xo, dx, xb8 >> 6); xb8 = nxt7(xb8); } while (0)
#define LDW(d, LO) do { dma((LO) ? wk2 : wk, wb8, wo, d, wb8 * 7); wb8 = nxt7(wb8); } while (0)
  if (w < 6) {
    dmai<L_XT>(wk, l16, mkdesc((const void*)parr), (64 * j + 2 * cl) * 512);
  } else {
    asm volatile("global_load_dword %0, %2, %3 sc1\n\tglobal_load_dword %1, %2, %4 sc1" : "=&v"(ee), "=&v"(en) : "v"(0), "s"(pech), "s"(pnon));
  }
  LDX(); LDW(dw1, L_W1); LDW(dw1, L_W1); LDX();
  const u32 ldw = ((const CAS u32*)ldt)[dch >> 1], dkw = ((const CAS u32*)dsk)[dch >> 1];
  u32 hoff;
  {
    const int g = ((8 * j + 2 * q + u) * 147) >> 11, sh = (g & 1) ? -1 - (g >> 1) : 1 + (g >> 1);
    const int t2 = T * 16 + c + sh, tw = t2 & 2047;
    hoff = (tw >> 4) * 14336 + q * 256 + (tw & 15) * 16 + (t2 == tw ? 0u : 0x80000000u);
  }
  const u32 xnto = (8 * w + (lane >> 3)) * 4096 + (lane & 7) * 16;
  {
    const v4u sv = {SENT, SENT, SENT, SENT};
    STWS(sv, hoff & 0x7fffffffu, O_HS + (2 * j + u) * 1024);
    STWS(sv, xnto, O_XNT + j * 262144 + cl * 128);
    STWS(sv, l16, O_X1 + (T * 14 + 2 * j + u) * 1024);
    if (q == 0) __builtin_amdgcn_raw_buffer_store_b64(v2u{SENT, SENT}, rs, c * 8, O_ST + ((T * 2 + u) * 7 + j) * 128, AUX_SC1);
#pragma unroll
    for (int s = 0; s < 4; ++s) STWS(sv, l16, O_H2 + ((cl * 4 + s) * 56 + j * 8 + w) * 1024);
  }
  u32 wa[4];
  {
    const int fsw = (pa >> 1) | ((q & 1) << 1);
    const u32 base = (16 * q + 8 * u + pa) * 128 + (((2 * pp) ^ fsw) << 4) + ((pp & 1) << 3);
#pragma unroll
    for (int p = 0; p < 4; ++p) {
      const int t = p ^ (2 * u) ^ (ti & 1);
      wa[p] = base ^ (((t >> 1) << 4) | ((t & 1) << 3));
    }
  }
  const bool sg = ((ti ^ q) & 1) != 0;
  const float dt = blo(pk(expf((dch & 1) ? bhi(ldw) : blo(ldw)), 0.f));
  const float dskip = (dch & 1) ? bhi(dkw) : blo(dkw);
  asm volatile("" : "+v"(wa[0]), "+v"(wa[1]), "+v"(wa[2]), "+v"(wa[3]), "+v"(hoff));

  f4 kp0;
  v4u KA[4];
  f2 p4, p8, g1;
  u32 non = 0;
  LAS char* const fw = S + L_C + w * FBS + lane * 4;
  LAS char* const fr = S + L_C + w * FBS + c * 288 + q * 16;
#define PUT(row, z) *(LAS u32*)(fw + (row) * 288) = pk2(z)
  {
    asm volatile("s_waitcnt vmcnt(12)" : "+v"(ee), "+v"(en)::"memory");
    if (w == 7) { non = (ee + 1 == en) ? ee + 2 : ee + 1; if (lane == 0) stflag(pnon, non); }
    BAR();
    STAMP(1);
    LAS u16* pl = (LAS u16*)(S + L_XT + ch * 512 + (64 * (w & 3) + lane) * 2);
    const float are = blo(pl[0]), aim = blo(pl[512]), bre = blo(pl[1024]), bim = blo(pl[1536]), cre = blo(pl[2048]), cim = blo(pl[2560]);
    LDX(); LDX();
    const float er = are * (dt * 1.4426950409f), ph = (dt * aim) * 0.15915494309f;
    const f2 cb = cmul(f2{cre, -cim}, f2{dt * bre, -dt * bim});
    const f2 p1 = cexp2(er, ph), p2 = cmul(p1, p1);
    p4 = cmul(p2, p2); p8 = cmul(p4, p4);
    const f2 p16 = cmul(p8, p8);
    g1 = cexp2(er * 256.f, ph * 256.f);
    SB(); LDX(); SB();
    {
      const f2 wc = {p16.x, -p16.y}, wc2 = cmul(wc, wc);
      f2 r0 = cb, r1 = cmul(cb, wc);
#pragma unroll
      for (int i = 0; i < 8; ++i) {
        PUT(2 * i, r0); PUT(2 * i + 1, r1);
        if (i < 7) { r0 = cmul(r0, wc2); r1 = cmul(r1, wc2); }
        if (i == 1 || i == 3) { SB(); LDX(); SB(); }
        if (i == 5) { SB(); LDW(dw1, L_W1); SB(); }
      }
    }
#pragma unroll
    for (int s = 0; s < 4; ++s) KA[s] = *(LAS v4u*)(fr + s * 64);
    CFENCE(); LDW(dw1, L_W1); SB();
    {
      f2 z = p1, y = p2;
#pragma unroll
      for (int i = 0; i < 4; ++i) {
        PUT(2 * i, z); PUT(2 * i + 1, cmul(z, p2)); PUT(8 + i, y); PUT(12 + i, cmul(y, g1));
        if (i < 3) { z = cmul(z, p4); y = cmul(y, p4); SB(); LDW(dw1, L_W1); SB(); }
      }
    }
    kp0 = f4{0, 0, 0, 0};
#pragma unroll
    for (int s = 0; s < 4; ++s) kp0 = mfma(KA[s], *(LAS v4u*)(fr + s * 64), kp0);
    CFENCE();
  }
  STAMP(2);

  v4u xpc, xnp;
  {
    VMWAIT(5);
    BAR();
    STAMP(3);
    v4u X[7];
    {
      LAS char* xr = S + L_W2 + (16 * ti + c) * 128 + (((2 * q + u) ^ (((c >> 1) + 6) & 7)) << 4);
#pragma unroll
      for (int b = 0; b < 7; ++b) X[b] = *(LAS v4u*)(xr + b * 8192);
      xpc = *(LAS v4u*)(xr + j * 8192);
    }
    f4 G = {0, 0, 0, 0}, Sx = {0, 0, 0, 0};
#pragma unroll
    for (int b = 0; b < 7; ++b) { G = mfma(X[b], X[b], G); Sx = mfma(ones, X[b], Sx); }
    const float gd = (c & 2) ? ((c & 1) ? G[3] : G[2]) : ((c & 1) ? G[1] : G[0]);
    if (q == (c >> 2)) *(LAS f2*)(S + L_ST + (ti * 16 + c) * 16 + u * 8) = f2{Sx[0], gd};
    VMWAIT(0);
    BAR();
    STAMP(4);
    if (w == 7) asm volatile("global_load_dword %0, %1, %2 sc1" : "=&v"(ee) : "v"(0), "s"(pech));
    f4 acc[4] = {{0, 0, 0, 0}, {0, 0, 0, 0}, {0, 0, 0, 0}, {0, 0, 0, 0}}, cs = {0, 0, 0, 0};
    const bool hi2 = (ti & 2) != 0;
    v4u A[2][4];
#pragma unroll
    for (int p = 0; p < 4; ++p) A[0][p] = trfrag(S + L_W1 + wa[p], 0, 512);
    SB();
#pragma unroll
    for (int b = 0; b < 7; ++b) {
      LDW(dw2, L_W2);
#pragma unroll
      for (int p4_ = 0; p4_ < 4; ++p4_) {
        const int p = (p4_ + 2) & 3;
        acc[p] = mfma(A[b & 1][p], X[b], acc[p]);
        if (b < 6) A[(b + 1) & 1][p] = trfrag(S + L_W1 + wa[p], (b + 1) * 8192, (b + 1) * 8192 + 512);
        SB();
      }
      const v4u s0 = A[b & 1][0], s2 = A[b & 1][2];
      cs = mfma(v4u{hi2 ? s2.x : s0.x, hi2 ? s2.y : s0.y, hi2 ? s2.z : s0.z, hi2 ? s2.w : s0.w}, ones, cs);
      SB();
    }
    *(LAS f4*)(S + L_C + w * FBS + l16) = acc[2];
    *(LAS f4*)(S + L_C + w * FBS + 1024 + l16) = acc[3];
    if (c == 0) *(LAS f4*)(S + L_CS + u * 256 + ((ti & 2) ^ (2 * u) ^ (ti & 1)) * 64 + q * 16) = cs;
    if (w == 7) {
      asm volatile("s_waitcnt vmcnt(7)" : "+v"(ee)::"memory");
NOUNROLL
      for (int spin = 0; spin < SPIN && !__all(ee == non); ++spin) { RETRY(0); ee = ldflag(pech); }
      if (lane == 0) { stflag(flags + F_CLR + cl * 8 + j, 1u); stflag(flags + F_ACK + blockIdx.x, 1u); }
    }
    BAR();
    STAMP(5);
    const f4 st = *(LAS f4*)(S + L_ST + (ti * 16 + c) * 16);
    const f4 o0 = acc[0] + *(LAS f4*)(S + L_C + (w ^ 1) * FBS + l16), o1 = acc[1] + *(LAS f4*)(S + L_C + (w ^ 1) * FBS + 1024 + l16);
    LAS char* cp = S + L_CS + (2 * u + (ti & 1)) * 64 + q * 16;
    LAS char* cp1 = S + L_CS + (2 * u + 1 - (ti & 1)) * 64 + q * 16;
    const f4 c0 = *(LAS f4*)(cp) + *(LAS f4*)(cp + 256), c1 = *(LAS f4*)(cp1) + *(LAS f4*)(cp1 + 256);
    const float mean = (st[0] + st[2]) * (1.f / 448.f);
    const float var = __builtin_fmaxf(__builtin_fmaf(st[1] + st[3], 1.f / 448.f, -mean * mean), 0.f);
    const float r1 = __builtin_amdgcn_rsqf(var + 1e-5f);
    const float rs2 = r1 * __builtin_amdgcn_rsqf(__builtin_fmaf(var * r1, r1, 1e-5f));
    const f2 vm = sp2(-mean), vr = sp2(rs2);
    const u32 a0 = gelu_pk(fma2(c0.xy, vm, o0.xy) * vr), a1 = gelu_pk(fma2(c0.zw, vm, o0.zw) * vr);
    const u32 b0 = gelu_pk(fma2(c1.xy, vm, o1.xy) * vr), b1 = gelu_pk(fma2(c1.zw, vm, o1.zw) * vr);
    v4u hv = {sg ? b0 : a0, sg ? b1 : a1, sg ? a0 : b0, sg ? a1 : b1};
    if ((int)hoff < 0) hv = v4u{0, 0, 0, 0};
    STWS(hv, hoff & 0x7fffffffu, O_HS + (2 * j + u) * 1024);
    STAMP(6);
    const f2 v1 = sp2(r1), vn = sp2(-mean * r1);
#pragma unroll
    for (int k = 0; k < 4; ++k) xnp[k] = pk2(fma2(unpk(xpc[k]), v1, vn));
  }

  v4u W3[28], H[7];
  u32 fl2;
  GAS u32* const pf2 = flags + F_CLR + (lane & 31) * 8 + j;
  const u32 w3b = O_W3T + (8 * j + w) * 28672, hsb = O_HS + (T * 14 + u) * 1024;
#define LDW3(k) W3[k] = LDWS(l16 + ((k) & 3) * 1024, w3b + ((k) >> 2) * 4096)
#define LDHS() _Pragma("unroll") for (int b = 0; b < 7; ++b) H[b] = LDWS(l16 + (b & 1) * 2048, hsb + (b >> 1) * 4096)
  const int tp = tid & 255;
  {
    LAS char* xt = S + L_XT + (16 * q + 8 * u) * 144 + (16 * ti + c) * 2;
#pragma unroll
    for (int k = 0; k < 4; ++k) {
      *(LAS u16*)(xt + (2 * k) * 144) = (u16)xnp[k];
      *(LAS u16*)(xt + (2 * k + 1) * 144) = (u16)(xnp[k] >> 16);
    }
    BAR();
    STWS(*(LAS v4u*)(S + L_XT + (8 * w + (lane >> 3)) * 144 + (lane & 7) * 16), xnto, O_XNT + j * 262144 + cl * 128);
    STAMP(7);
    const f2 g2 = cmul(g1, g1), g3 = cmul(g2, g1), p12 = cmul(p4, p8);
    PUT(0, p4); PUT(1, p12); PUT(2, cmul(p4, g1)); PUT(3, cmul(p12, g1)); PUT(4, cmul(p4, g2)); PUT(5, cmul(p12, g2));
    PUT(6, cmul(p4, g3)); PUT(7, cmul(p12, g3));
    PUT(8, (f2{1.f, 0.f})); PUT(9, p8); PUT(10, g1); PUT(11, cmul(p8, g1)); PUT(12, g2); PUT(13, cmul(p8, g2)); PUT(14, g3); PUT(15, cmul(p8, g3));
    v4u B[4];
#pragma unroll
    for (int s = 0; s < 4; ++s) B[s] = *(LAS v4u*)(fr + s * 64);
    CFENCE();
    GAS u32* const pf1 = lane < 24 ? flags + F_CLR + ((cl + (lane >> 3) - 1) & 31) * 8 + min(lane & 7, 6) : flags + F_W3 + 4 * j + ti;
    const u32 fl1 = ldflag(pf1);
    SB();
    {
      f2 z = cmul(g2, g2);
#pragma unroll
      for (int i = 0; i < 4; ++i) { PUT(2 * i, z); PUT(2 * i + 1, cmul(z, p8)); z = cmul(z, g1); }
      PUT(8, z);
    }
    f4 kp1 = {0, 0, 0, 0}, kp2 = {0, 0, 0, 0};
#pragma unroll
    for (int s = 0; s < 4; ++s) kp1 = mfma(KA[s], B[s], kp1);
#pragma unroll
    for (int s = 0; s < 4; ++s) kp2 = mfma(KA[s], *(LAS v4u*)(fr + s * 64), kp2);
    CFENCE();
    LAS char* pw = S + L_C + w * FBS + c * 64 + q * 16;
    *(LAS f4*)(pw) = kp0; *(LAS f4*)(pw + 1024) = kp1; *(LAS f4*)(pw + 2048) = kp2;
    *(LAS v4u*)(S + L_C + w * FBS + 3072 + l16) = xpc;
    if (tid < 32) *(LAS v4u*)(S + L_KC + (tid >> 2) * KCS + 4096 + (tid & 3) * 16) = v4u{0, 0, 0, 0};
    else if (tid < 160) *(LAS v4u*)(S + L_U + ((tid - 32) >> 4) * 1280 + (tid & 15) * 16) = v4u{0, 0, 0, 0};
    else if (tid < 162) *(LAS v2u*)(S + L_KT + (tid - 160) * KTS) = v2u{0, 0};
    STAMP(8);
    wait_flags(pf1, fl1);
    VMWAIT(0);
    LDHS();
    fl2 = ldflag(pf2);
    SB();
    STAMP(9);
    BAR();
    if (tp < 164) {
      LAS char* pp4 = S + L_C + ch * 4 * FBS + tp * 16;
      const f4 v = (*(LAS f4*)(pp4) + *(LAS f4*)(pp4 + FBS)) + (*(LAS f4*)(pp4 + 2 * FBS) + *(LAS f4*)(pp4 + 3 * FBS));
      const int col = tp >> 2, k8 = col & 7;
      const int cv = col < 8 ? 2 * col + 1 : col < 16 ? 2 + 4 * (col & 3) + 64 * (k8 & 4) : 8 * (col & 1) + (col < 24 ? 4 + 128 * (k8 & 6) : 128 * ((col - 24) & 30));
      LAS char* kt = S + L_KT + ch * KTS + 8 + cv * 2 + (tp & 3) * 128;
      const u32 k01 = pk(v[0], v[1]), k23 = pk(v[2], v[3]);
      if (tp <= 160) *(LAS u16*)(kt) = (u16)k01;
      if (tp < 160) { *(LAS u16*)(kt + 32) = (u16)(k01 >> 16); *(LAS u16*)(kt + 64) = (u16)k23; *(LAS u16*)(kt + 96) = (u16)(k23 >> 16); }
    }
    SB(); LDW3(0); LDW3(1); SB();
    int ga[11];
    {
      const float m0 = (float)(2047 - 8 * tp);
#pragma unroll
      for (int e = 0; e < 12; e += 2) {
        const u32 r = pk(m0 - (float)e, m0 - (float)(e + 1));
        ga[e] = 2 * (int)blo(r);
        if (e < 10) ga[e + 1] = 2 * (int)bhi(r);
      }
    }
    BAR();
    {
      u32 v[11], pr[10];
#pragma unroll
      for (int e = 0; e < 11; ++e) v[e] = *(LAS u16*)(S + L_KT + 8 + ch * KTS + ga[e]);
      SB(); LDW3(2); LDW3(3); SB();
#pragma unroll
      for (int e = 0; e < 10; ++e) pr[e] = v[e] | (v[e + 1] << 16);
#pragma unroll
      for (int k = 0; k < 4; ++k) *(LAS v4u*)(S + L_KC + (ch * 4 + k) * KCS + tp * 16) = v4u{pr[3 - k], pr[5 - k], pr[7 - k], pr[9 - k]};
    }
    STAMP(10);
  }

  f4 cs3a, cs3b;
  {
    v4u A[2][4];
#pragma unroll
    for (int p = 0; p < 4; ++p) A[0][p] = trfrag(S + L_W2 + wa[p], 0, 512);
    SB();
    wait_flags(pf2, fl2);
    v4u uv = LDWS(tp * 16, O_XNT + dch * 4096);
    SB();
    f4 acc[4] = {{0, 0, 0, 0}, {0, 0, 0, 0}, {0, 0, 0, 0}, {0, 0, 0, 0}};
#pragma unroll
    for (int b = 0; b < 7; ++b) {
#pragma unroll
      for (int p4_ = 0; p4_ < 4; ++p4_) {
        const int p = (p4_ + 2) & 3;
        acc[p] = mfma(A[b & 1][p], H[b], acc[p]);
        if (b < 6) A[(b + 1) & 1][p] = trfrag(S + L_W2 + wa[p], (b + 1) * 8192, (b + 1) * 8192 + 512);
        if (p4_ == 3) LDW3(4 + b);
        SB();
      }
    }
    if (__any(max3u(max3u(H[0].x, H[1].x, H[2].x), max3u(H[3].x, H[4].x, H[5].x), H[6].x) == SENT)) {
NOUNROLL
      for (int spin = 0; spin < SPIN; ++spin) {
        RETRY(3);
        CFENCE();
        LDHS();
        if (!__any(max3u(max3u(H[0].x, H[1].x, H[2].x), max3u(H[3].x, H[4].x, H[5].x), H[6].x) == SENT)) break;
      }
#pragma unroll
      for (int p = 0; p < 4; ++p) {
        acc[p] = f4{0, 0, 0, 0};
#pragma unroll
        for (int b = 0; b < 7; ++b) acc[p] = mfma(trfrag(S + L_W2 + wa[p], b * 8192, b * 8192 + 512), H[b], acc[p]);
      }
    }
    *(LAS f4*)(S + L_C + w * FBS + l16) = acc[2];
    *(LAS f4*)(S + L_C + w * FBS + 1024 + l16) = acc[3];
NOUNROLL
    for (int spin = 0; spin < SPIN && __any(uv.x == SENT); ++spin) { RETRY(4); CFENCE(); uv = LDWS(tp * 16, O_XNT + dch * 4096); }
    *(LAS v4u*)(S + L_U + ch * 5120 + (tp & 3) * 1280 + 256 + (tp >> 2) * 16) = uv;
    BAR();
    STAMP(11);
    const f4 o0 = acc[0] + *(LAS f4*)(S + L_C + (w ^ 1) * FBS + l16), o1 = acc[1] + *(LAS f4*)(S + L_C + (w ^ 1) * FBS + 1024 + l16);
    const u32 n0 = sg ? xnp[2] : xnp[0], n1 = sg ? xnp[3] : xnp[1], n2 = sg ? xnp[0] : xnp[2], n3 = sg ? xnp[1] : xnp[3];
    const u32 a0 = pk2(o0.xy + unpk(n0)), a1 = pk2(o0.zw + unpk(n1)), b0 = pk2(o1.xy + unpk(n2)), b1 = pk2(o1.zw + unpk(n3));
    const v4u x1p = v4u{sg ? b0 : a0, sg ? b1 : a1, sg ? a0 : b0, sg ? a1 : b1};
    STWS(x1p, l16, O_X1 + (T * 14 + 2 * j + u) * 1024);
    *(LAS v4u*)(S + L_C + w * FBS + 2048 + l16) = x1p;
    const f2 e0 = unpk(a0), e1 = unpk(a1), e2 = unpk(b0), e3 = unpk(b1);
    const f2 t1 = (e0 + e1) + (e2 + e3), t2 = fma2(e0, e0, fma2(e1, e1, fma2(e2, e2, e3 * e3)));
    float s1 = t1.x + t1.y, s2 = t2.x + t2.y;
    s1 += __shfl_xor(s1, 16); s2 += __shfl_xor(s2, 16);
    s1 += __shfl_xor(s1, 32); s2 += __shfl_xor(s2, 32);
    if (q == 0) __builtin_amdgcn_raw_buffer_store_b64(v2u{asu(s1), asu(s2)}, rs, c * 8, O_ST + ((T * 2 + u) * 7 + j) * 128, AUX_SC1);
    SB(); LDW3(11); SB();
    STAMP(12);
  }

  GAS u32* const pf4 = flags + F_W4 + 4 * min(lane & 7, 6) + ti;
  u32 fl4;
  {
    const int rho = (w >> 1) & 1;
    v2u sa, sb;
    const v4u dws = mkdesc(ws);
    const u32 x1b = O_X1 + (T * 14 + u) * 1024, x1l = L_W2 + (ti * 14 + u) * 1024;
#define LDX1() do { \
      unroll<7>([&](auto b) { dmai_sc1<b * 2048>(sb0 + x1l, l16, dws, x1b + b * 2048); }); \
      sa = __builtin_amdgcn_raw_buffer_load_b64(rs, q * 128 + c * 8, O_ST + (T * 2 + u) * 896, AUX_SC1); \
      sb = __builtin_amdgcn_raw_buffer_load_b64(rs, min(q + 4, 6) * 128 + c * 8, O_ST + (T * 2 + u) * 896, AUX_SC1); } while (0)
    LAS char* pa_ = S + L_KC + (ch * 4 + (c & 3)) * KCS + 2 * (2044 - (c & 12) + 8 * q - 16 * rho - 32 * u) - 3072 - 896;
    LAS char* pb_ = S + L_U + ch * 5120 + q * 1280 + (16 + c - u) * 16 - 224;
    f4 y[4] = {{0, 0, 0, 0}, {0, 0, 0, 0}, {0, 0, 0, 0}, {0, 0, 0, 0}};
    v4u B[2][4], A[2][4];
#define CLDB(it, a) B[(it) & 1][a] = *(LAS v4u*)(pb_ + (7 - (it)) * 32 + (a) * 256)
#define CLDA(it, a) do { const v2u lo = *(volatile LAS v2u*)(pa_ + (7 - (it)) * 128 + (3 - (a)) * 1024), hi = *(volatile LAS v2u*)(pa_ + (7 - (it)) * 128 + (3 - (a)) * 1024 + 8); \
      A[(it) & 1][a] = v4u{lo.x, lo.y, hi.x, hi.y}; } while (0)
#pragma unroll
    for (int a = 0; a < 4; ++a) { CLDB(0, a); CLDA(0, a); }
    SB();
#pragma unroll
    for (int it = 0; it < 8; ++it) {
      int n = 0;
#pragma unroll
      for (int a = 0; a < 4; ++a)
#pragma unroll
        for (int k = a; k < 4; ++k) {
          y[k] = mfma(A[it & 1][a], B[it & 1][k - a], y[k]);
          if (it < 7 && n < 8) { if (n & 1) CLDB(it + 1, n >> 1); else CLDA(it + 1, n >> 1); }
          if (n == 8) LDW3(12 + 2 * it);
          if (n == 9) LDW3(13 + 2 * it);
          SB();
          ++n;
        }
      if (it == XIT) { LDX1(); SB(); }
    }
    cs3a = __builtin_bit_cast(f4, LDWS(q * 32, O_CS3 + (8 * j + w) * 128));
    cs3b = __builtin_bit_cast(f4, LDWS(q * 32 + 16, O_CS3 + (8 * j + w) * 128));
    fl4 = ldflag(pf4);
    SB();
    STAMP(13);
    const f4 e0 = u ? y[0] : y[2], e1 = u ? y[1] : y[3], k0 = u ? y[2] : y[0], k1 = u ? y[3] : y[1];
    *(LAS f4*)(S + L_C + w * FBS + l16) = e0;
    *(LAS f4*)(S + L_C + w * FBS + 1024 + l16) = e1;
    BAR();
#pragma unroll
    for (int k = 0; k < 2; ++k) {
      const f4 v = (k ? k1 : k0) + *(LAS f4*)(S + L_C + (w ^ 1) * FBS + k * 1024 + l16);
      const int blk = 32 * u + c;
      const v2u uu = *(volatile LAS v2u*)(S + L_U + ch * 5120 + (2 * rho + (q >> 1)) * 1280 + (16 + blk) * 16 + (q & 1) * 8 + k * 256);
      *(LAS v2u*)(S + L_KT + ch * 4096 + blk * 64 + rho * 32 + q * 8 + k * 1024) =
          v2u{pk2(fma2(unpk(uu.x), sp2(dskip), v.xy)), pk2(fma2(unpk(uu.y), sp2(dskip), v.zw))};
    }
    STAMP(14);
NOUNROLL
    for (int spin = 0; spin < SPIN; ++spin) {
      asm volatile("s_waitcnt vmcnt(0)" : "+v"(sa), "+v"(sb)::"memory");
      u32 m = max(sa.x, sb.x);
#pragma unroll
      for (int b = 0; b < 7; ++b) m = max(m, *(LAS u32*)(S + x1l + b * 2048 + l16));
      if (!__any(m == SENT)) break;
      RETRY(5);
      LDX1();
    }
    {
      float s1 = asf(sa.x) + (q < 3 ? asf(sb.x) : 0.f), s2 = asf(sa.y) + (q < 3 ? asf(sb.y) : 0.f);
      s1 += __shfl_xor(s1, 16); s2 += __shfl_xor(s2, 16);
      s1 += __shfl_xor(s1, 32); s2 += __shfl_xor(s2, 32);
      if (q == 0) *(LAS f2*)(S + L_ST + (ti * 16 + c) * 16 + u * 8) = f2{s1, s2};
    }
    STAMP(15);
    BAR();
    STWS(*(LAS v4u*)(S + L_KT + tid * 16), tp * 16, O_ST2 + dch * 4096);
    wait_flags(pf4, fl4);
    STAMP(16);
  }

  v4u W4[28], own[4], H2[4][6];
  int bo[7];
  bo[0] = j * 8192;
#pragma unroll
  for (int b = 1; b < 7; ++b) bo[b] = nxt7(bo[b - 1]);
  const u32 w4b = O_W4T + (j * 8 + w) * 28672, h2b = O_H2 + cl * 229376 + w * 1024;
#define LDW4(k) W4[k] = LDWS(l16 + ((k) & 3) * 1024, w4b + (bo[(k) >> 2] >> 1))
#define LDH2(s) _Pragma("unroll") for (int b = 1; b < 7; ++b) H2[s][b - 1] = LDWS(l16, h2b + ((ti + 1 + (s)) & 3) * 57344 + bo[b])
#define CKH2(s) NOUNROLL for (int spin = 0; spin < SPIN && __any(max3u(max3u(H2[s][0].x, H2[s][1].x, H2[s][2].x), max3u(H2[s][3].x, H2[s][4].x, H2[s][5].x), 0u) == SENT); ++spin) { \
    RETRY(6); CFENCE(); LDH2(s); }
  if (w < 4) __builtin_amdgcn_s_setprio(1);
  v4u Bq[4];
#pragma unroll
  for (int k = 0; k < 3; ++k) Bq[k] = *(LAS v4u*)(S + L_W2 + ((ti + 1) & 3) * 14336 + l16 + k * 1024);
#pragma unroll
  for (int s = 0; s < 4; ++s) {
    const int tl = (ti + 1 + s) & 3;
    LAS char* ap = S + L_W2 + tl * 14336 + l16;
    LAS char* an = S + L_W2 + ((tl + 1) & 3) * 14336 + l16;
    const f4 st = *(LAS f4*)(S + L_ST + (tl * 16 + c) * 16);
    f4 a0 = {0, 0, 0, 0}, a1 = {0, 0, 0, 0};
#pragma unroll
    for (int ks = 0; ks < 14; ++ks) {
      const int qi = (s * 14 + ks) & 3, qn = (s * 14 + ks + 3) & 3;
      a0 = mfma(W3[2 * ks], Bq[qi], a0);
      if (ks + 3 < 14) Bq[qn] = *(LAS v4u*)(ap + (ks + 3) * 1024);
      else if (s < 3) Bq[qn] = *(LAS v4u*)(an + (ks - 11) * 1024);
      SB();
      a1 = mfma(W3[2 * ks + 1], Bq[qi], a1);
      if (s < 3 && ks % 3 == 0 && s * 5 + ks / 3 < 14) LDW4(s * 5 + ks / 3);
      if (s == 3) LDW4(14 + ks);
      SB();
    }
    if (s == 0) {
      VMWAIT(5);
      if (lane == 0) stflag(flags + F_S + j * 256 + (w & 3) * 64 + cl * 2 + ch, 1u);
    }
    const float mean = (st[0] + st[2]) * (1.f / 448.f);
    const f2 vr = sp2(__builtin_amdgcn_rsqf(__builtin_fmaxf(__builtin_fmaf(st[1] + st[3], 1.f / 448.f, -mean * mean), 0.f) + 1e-5f)), vm = sp2(-mean);
    own[s] = v4u{gelu_pk(fma2(cs3a.xy, vm, a0.xy) * vr), gelu_pk(fma2(cs3a.zw, vm, a0.zw) * vr),
                 gelu_pk(fma2(cs3b.xy, vm, a1.xy) * vr), gelu_pk(fma2(cs3b.zw, vm, a1.zw) * vr)};
    STWS(own[s], l16, h2b + tl * 57344 + bo[0]);
    SB();
    if (s >= 2) { LDH2(s - 2); SB(); }
    STAMP(17 + s);
  }
  GAS u32* const pfs = flags + F_S + j * 256 + (cl >> 3) * 64 + lane;
  const u32 fls = ldflag(pfs);
  f4 o[4][4];
#pragma unroll
  for (int s = 0; s < 4; ++s)
#pragma unroll
    for (int t = 0; t < 4; ++t) o[s][t] = mfma(W4[t], own[s], f4{0, 0, 0, 0});
  SB();
  STAMP(21);
  f4 pre0 = {0, 0, 0, 0}, pre1 = {0, 0, 0, 0};
#pragma unroll
  for (int s = 0; s < 4; ++s) {
    const int tl = (ti + 1 + s) & 3;
    CKH2(s);
#pragma unroll
    for (int b = 1; b < 7; ++b) {
      f4 r0, r1;
      if (s == 3) {
        r0 = *(LAS f4*)(S + (ti * 7 + b - 1) * 4096 + u * 2048 + l16);
        r1 = *(LAS f4*)(S + (ti * 7 + b - 1) * 4096 + u * 2048 + 1024 + l16);
      }
#pragma unroll
      for (int t = 0; t < 4; ++t) o[s][t] = mfma(W4[4 * b + t], H2[s][b - 1], o[s][t]);
      if (s == 3) { pre0 += r0; pre1 += r1; }
      SB();
    }
    if (s < 2) { LDH2(s + 2); SB(); }
    if (s == 0) {
      wait_flags(pfs, fls);
      dmai_sc1<L_XT>(wk, (lane >> 3) * 4096 + (lane & 7) * 16, mkdesc(ws), O_ST2 + (64 * j + 8 * w) * 4096 + cl * 128);
      BAR();
    }
    if (s < 3) {
#pragma unroll
      for (int t = 0; t < 4; ++t) *(LAS f4*)(S + (tl * 7 + 2 * s + u) * 4096 + t * 1024 + l16) = o[s][t];
    }
    if (s == 2) { VMWAIT(6); BAR(); }
    STAMP(22 + s);
  }
  {
    const f4 e0 = u ? o[3][0] : o[3][2], e1 = u ? o[3][1] : o[3][3], k0 = u ? o[3][2] : o[3][0], k1 = u ? o[3][3] : o[3][1];
    *(LAS f4*)(S + (ti * 7 + 6) * 4096 + (u ^ 1) * 2048 + l16) = e0;
    *(LAS f4*)(S + (ti * 7 + 6) * 4096 + (u ^ 1) * 2048 + 1024 + l16) = e1;
    const v4u xq = *(LAS v4u*)(S + L_C + w * FBS + 3072 + l16), x1p = *(LAS v4u*)(S + L_C + w * FBS + 2048 + l16);
    u32 lz = l16;
    asm volatile("" : "+v"(lz));
    const v4u sp_ = trfrag(S + L_XT + u * 1024 + ti * 32 + (lz >> 8) * 2048 + ((lz >> 6) & 3) * 128 + ((lz >> 4) & 3) * 8, 0, 512);
    const f4 q0 = k0 + pre0, q1 = k1 + pre1;
    BAR();
    const f4 v0 = q0 + *(LAS f4*)(S + (ti * 7 + 6) * 4096 + u * 2048 + l16), v1 = q1 + *(LAS f4*)(S + (ti * 7 + 6) * 4096 + u * 2048 + 1024 + l16);
    v4u r;
#pragma unroll
    for (int k = 0; k < 4; ++k) {
      const f2 v = (k == 0 ? v0.xy : k == 1 ? v0.zw : k == 2 ? v1.xy : v1.zw) + unpk(x1p[k]);
      r[k] = pk2(fma2(unpk(sp_[k]), v * sigm2(v * sp2(-1.4426950409f)), unpk(xq[k])));
    }
    __builtin_amdgcn_raw_buffer_store_b128(r, __builtin_amdgcn_make_buffer_rsrc(out, 0, 1835008, 0x00020000), ((lz >> 4) & 15) * 896 + (lz >> 8) * 32, T * 14336 + 128 * j + 16 * u, AUX_SC1);
    STAMP(26);
    DUMP();
  }
}

extern "C" void kernel_launch(void* const* d_in, const int* in_sizes, int n_in, void* d_out, int out_size, void* d_ws, size_t ws_size,
                              hipStream_t stream) {
  fused_kernel<<<256, 512, 0, stream>>>((const u16*)d_in[0], (const u16*)d_in[1], (const u16*)d_in[2], (const u16*)d_in[3], (const u16*)d_in[4],
                                        (const u16*)d_in[5], (const u16*)d_in[6], (const u16*)d_in[7], (const u16*)d_in[8], (const u16*)d_in[10],
                                        (const u16*)d_in[11], (const u16*)d_in[12], (const u16*)d_in[13], (u16*)d_out, (unsigned char*)d_ws);
}
```

```cpp
#define EVK 99
#include <hip/hip_runtime.h>
#include <type_traits>
typedef unsigned short u16;
typedef unsigned u32;
typedef unsigned long long u64;
typedef u32 v4u __attribute__((ext_vector_type(4)));
typedef u32 v2u __attribute__((ext_vector_type(2)));
typedef float f4 __attribute__((ext_vector_type(4)));
typedef float f2 __attribute__((ext_vector_type(2)));
typedef short s8v __attribute__((ext_vector_type(8)));
typedef short s4v __attribute__((ext_vector_type(4)));
typedef __bf16 bf2 __attribute__((ext_vector_type(2)));
#define LAS __attribute__((address_space(3)))
#define GAS __attribute__((address_space(1)))
#define CAS __attribute__((address_space(4)))
#define RLX_AGENT __ATOMIC_RELAXED, __HIP_MEMORY_SCOPE_AGENT
#define DEV __device__ __forceinline__
#define SB() __builtin_amdgcn_sched_barrier(0)
#define CFENCE() asm volatile("" ::: "memory")
#define VMWAIT(n) asm volatile("s_waitcnt vmcnt(" #n ")" ::: "memory")
#define BAR() asm volatile("s_waitcnt lgkmcnt(0)\n\ts_barrier" ::: "memory")
#define NOUNROLL _Pragma("clang loop unroll(disable)")

constexpr u32 F_S = 0, F_W3 = 1792, F_W4 = 1824, F_CLR = 1856, F_ACK = 2112;
constexpr u32 O_NON = 32768, O_ECH = 65536;
constexpr u32 O_HS = 131072, O_X1 = O_HS + 1835008, O_ST = O_X1 + 1835008, O_H2 = O_ST + 229376, O_XNT = O_H2 + 7340032,
              O_ST2 = O_XNT + 1835008, O_W3T = O_ST2 + 1835008, O_W4T = O_W3T + 1605632, O_CS3 = O_W4T + 1605632,
              O_DBG = O_CS3 + 8192, WS_END = O_DBG + (1 << 20);
constexpr u32 L_W1 = 0, L_W2 = 57344, L_C = 114688, FBS = 4608, L_XT = L_C + 8 * FBS, L_ST = L_XT + 9216, L_CS = L_ST + 1024;
constexpr u32 L_KC = 0, KCS = 4160, L_U = 33280, L_KT = 43520, KTS = 4128;
constexpr int AUX_SC1 = 16, SPIN = 1 << 15;
constexpr u32 SENT = 0xffffffffu;
#define XIT 1

#define SPM 100
#define STAMP(k) do { if ((k) == EVK) { SB(); tev_ = __builtin_amdgcn_s_memrealtime(); SB(); } } while (0)
#define CSTAMP(k)
#define DUMP()
#define RETRY(k)

DEV f4 mfma(v4u a, v4u b, f4 c) { return __builtin_amdgcn_mfma_f32_16x16x32_bf16(__builtin_bit_cast(s8v, a), __builtin_bit_cast(s8v, b), c, 0, 0, 0); }
DEV u32 pk2(f2 a) { return __builtin_bit_cast(u32, __builtin_convertvector(a, bf2)); }
DEV u32 pk(float a, float b) { return pk2(f2{a, b}); }
DEV float asf(u32 v) { return __builtin_bit_cast(float, v); }
DEV u32 asu(float v) { return __builtin_bit_cast(u32, v); }
DEV float blo(u32 v) { return asf(v << 16); }
DEV float bhi(u32 v) { return asf(v & 0xffff0000u); }
DEV f2 unpk(u32 v) { return f2{blo(v), bhi(v)}; }
DEV f2 sp2(float v) { return f2{v, v}; }
DEV f2 fma2(f2 a, f2 b, f2 c) { return __builtin_elementwise_fma(a, b, c); }
DEV f2 sigm2(f2 t) {
  const f2 d = f2{__builtin_amdgcn_exp2f(t.x), __builtin_amdgcn_exp2f(t.y)} + sp2(1.0f);
  return f2{__builtin_amdgcn_rcpf(d.x), __builtin_amdgcn_rcpf(d.y)};
}
DEV u32 gelu_pk(f2 z) { return pk2(z * sigm2(z * fma2(z * z, sp2(-0.1029432397f), sp2(-2.302208198f)))); }
DEV v2u trd(LAS char* p) { return __builtin_bit_cast(v2u, __builtin_amdgcn_ds_read_tr16_b64_v4i16((LAS s4v*)p)); }
DEV v4u trfrag(LAS char* p, int o0, int o1) { const v2u a = trd(p + o0), b = trd(p + o1); return v4u{a.x, a.y, b.x, b.y}; }
DEV v4u mkdesc(const void* p) { const u64 a = (u64)p; return v4u{(u32)a, (u32)(a >> 32) & 0xffffu, 0x7fffffffu, 0x20000u}; }
DEV void dma(u32 la, u32 lb, u32 voff, v4u d, u32 soff) {
  asm volatile("s_add_u32 m0, %0, %1\n\ts_nop 0\n\tbuffer_load_dwordx4 %2, %3, %4 offen lds" ::"s"(la), "s"(lb), "v"(voff), "s"(d), "s"(soff) : "scc");
}
template <int LB> DEV void dmai(u32 la, u32 voff, v4u d, u32 soff) {
  asm volatile("s_add_u32 m0, %0, %1\n\ts_nop 0\n\tbuffer_load_dwordx4 %2, %3, %4 offen lds" ::"s"(la), "i"(LB), "v"(voff), "s"(d), "s"(soff) : "scc");
}
template <int LB> DEV void dmai_sc1(u32 la, u32 voff, v4u d, u32 soff) {
  asm volatile("s_add_u32 m0, %0, %1\n\ts_nop 0\n\tbuffer_load_dwordx4 %2, %3, %4 offen sc1 lds" ::"s"(la), "i"(LB), "v"(voff), "s"(d), "s"(soff) : "scc");
}
template <int N, class F> DEV void unroll(F f) { if constexpr (N > 0) { unroll<N - 1>(f); f(std::integral_constant<int, N - 1>{}); } }
DEV u32 ldflag(GAS u32* p) { return __hip_atomic_load(p, RLX_AGENT); }
DEV void stflag(GAS u32* p, u32 v) { __hip_atomic_store(p, v, RLX_AGENT); }
DEV void wait_flags(GAS u32* p, u32 v) {
NOUNROLL
  for (int spin = 0; spin < SPIN && !__all(v != 0u); ++spin) v = ldflag(p);
  __builtin_amdgcn_fence(__ATOMIC_ACQUIRE, "wavefront");
}
DEV u32 max3u(u32 a, u32 b, u32 c) { return max(max(a, b), c); }
DEV f2 cmul(f2 a, f2 b) {
  f2 r;
  asm("v_pk_mul_f32 %0, %1, %2 op_sel:[0,0] op_sel_hi:[0,1]\n\tv_pk_fma_f32 %0, %1, %2, %0 op_sel:[1,1,0] op_sel_hi:[1,0,1] neg_lo:[0,1,0]"
      : "=&v"(r) : "v"(a), "v"(b));
  return r;
}
DEV f2 cexp2(float er, float ph) {
  const float f = __builtin_amdgcn_fractf(ph);
  return sp2(__builtin_amdgcn_exp2f(er)) * f2{__builtin_amdgcn_cosf(f), __builtin_amdgcn_sinf(f)};
}
DEV int nxt7(int b8) { return b8 == 6 * 8192 ? 0 : b8 + 8192; }

__global__ void __launch_bounds__(512, 2)
fused_kernel(const u16*, const u16*, const u16*, const u16*, const u16*, const u16*, const u16*, const u16*, const u16*, const u16*,
             const u16*, const u16*, const u16*, u16*, unsigned char*) {
  __shared__ __attribute__((aligned(16))) char smem[163840];
  LAS char* const S = (LAS char*)smem;
  const u32 sb0 = (u32)(size_t)S;
  const int tid = threadIdx.x, lane = tid & 63, w = __builtin_amdgcn_readfirstlane(tid >> 6);
  u64 tstart_; asm volatile("s_memrealtime %0\n s_waitcnt lgkmcnt(0)" : "=s"(tstart_) :: "memory");
  typedef u64 u64x8 __attribute__((ext_vector_type(8)));
  u64x8 ka;
  u64 px, parr;
  asm volatile("s_load_dwordx16 %0, %3, 0x38\n\ts_load_dwordx2 %1, %3, 0x0\n\ts_load_dwordx2 %2, %3, %4 offset:0x8"
               : "=&s"(ka), "=&s"(px), "=&s"(parr) : "s"(__builtin_amdgcn_kernarg_segment_ptr()), "s"(min(w, 5) * 8));
  const int c = lane & 15, q = lane >> 4, pa = (lane >> 2) & 3, pp = lane & 3;
  const int xcd = blockIdx.x & 7, rk = blockIdx.x >> 3;
  int cq = ((rk - 4) * 37) >> 8, j = (rk - 4) - 7 * cq, cl = xcd * 4 + cq;
  u32 wk = sb0 + w * 1024;
  const u32 rowo = (8 * w + (lane >> 3)) * 896;
  u32 xo = rowo + 16 * ((lane & 7) ^ ((4 * w + q + 6) & 7)), wo = rowo + 16 * ((lane & 7) ^ ((q & 1) | (w & 2))), l16 = lane * 16;
  asm volatile("" : "+v"(xo), "+v"(wo), "+v"(l16));
  asm volatile("s_waitcnt lgkmcnt(0)" : "+s"(ka), "+s"(px), "+s"(parr), "+s"(wk), "+s"(cq), "+s"(j), "+s"(cl));
  const char *x = (const char*)px, *w1 = (const char*)ka[2], *w2 = (const char*)ka[3], *w3 = (const char*)ka[4], *w4 = (const char*)ka[5];
  const u16 *ldt = (const u16*)ka[0], *dsk = (const u16*)ka[1];
  u16* const out = (u16*)ka[6];
  unsigned char* const ws = (unsigned char*)ka[7];
  GAS u32* const flags = (GAS u32*)ws;
  GAS u32* const pnon = (GAS u32*)(ws + O_NON) + blockIdx.x * 32;
  GAS u32* const pech = (GAS u32*)(ws + O_ECH) + blockIdx.x * 32;
  const __amdgpu_buffer_rsrc_t rs = __builtin_amdgcn_make_buffer_rsrc(ws, 0, WS_END, 0x00020000);
  const v4u ones = {0x3f803f80u, 0x3f803f80u, 0x3f803f80u, 0x3f803f80u};
#define LDWS(vo, so) __builtin_amdgcn_raw_buffer_load_b128(rs, vo, so, AUX_SC1)
#define STWS(v, vo, so) do { const v4u sv_ = (v); __builtin_amdgcn_raw_buffer_store_b128(sv_, rs, vo, so, AUX_SC1); asm volatile("s_nop 1" ::"v"(sv_) : "memory"); } while (0)
  u64 tev_ = tstart_;
  CSTAMP(30);
  STAMP(0);

  if (rk < 4) {
    const int sp = xcd * 4 + rk;
    STAMP(4 + SPM);
    if (sp >= 28) {
      if (sp > 28) return;
      STWS((v4u{0, 0, 0, 0}), tid * 16, 0);
      STWS((v4u{0, 0, 0, 0}), tid * 16, 8192);
      VMWAIT(0);
      BAR();
      if (w >= 4) return;
      const int b = w * 64 + lane;
      const bool idle = b < 32 && (b & 7) == 7;
      u32 last = 0;
NOUNROLL
      for (int spin = 0; spin < 4 * SPIN; ++spin) {
        const u32 v = ldflag((GAS u32*)(ws + O_NON) + b * 32), a = ldflag(flags + F_ACK + b);
        if (v != last || spin == 0) stflag((GAS u32*)(ws + O_ECH) + b * 32, v);
        last = v;
        if (__all(a != 0u || idle)) break;
      }
      return;
    }
    if (w == 7) {
      const u32 e = ldflag(pech), n = ldflag(pnon), non = (e + 1 == n) ? e + 2 : e + 1;
      if (lane == 0) stflag(pnon, non);
      BAR();
NOUNROLL
      for (int spin = 0; spin < SPIN && !__all(ldflag(pech) == non); ++spin) {}
      if (lane == 0) stflag(flags + F_ACK + blockIdx.x, 1u);
      BAR();
      return;
    }
    const u32 lb = sb0 + w * 16384;
    const v4u d3 = mkdesc((const char*)w3 + w * 64 * 3584 + sp * 128), d4 = mkdesc((const char*)w4 + sp * 64 * 896 + w * 128);
    const u32 v3 = (lane >> 3) * 3584 + (lane & 7) * 16, v4 = (lane >> 3) * 896 + (lane & 7) * 16;
    unroll<8>([&](auto g) { dmai<((2 * g & 7) + (g >> 2)) * 1024>(lb, v3, d3, ((2 * g & 7) + (g >> 2)) * 8 * 3584); });
    {
      LAS char* p = S + w * 16384 + (16 * q + pa) * 128 + 16 * pp;
      f4 cs[4] = {{0, 0, 0, 0}, {0, 0, 0, 0}, {0, 0, 0, 0}, {0, 0, 0, 0}};
#pragma unroll
      for (int u = 0; u < 2; ++u) {
        VMWAIT(4);
        if (u == 0) STAMP(1 + SPM);
        v4u a[4];
#pragma unroll
        for (int et = 0; et < 4; ++et) a[et] = trfrag(p, u * 1024 + 64 * (et >> 1) + 8 * (et & 1), u * 1024 + 64 * (et >> 1) + 8 * (et & 1) + 512);
#pragma unroll
        for (int et = 0; et < 4; ++et) {
          STWS(a[et], l16, O_W3T + (((2 * sp + (et >> 1)) * 14 + 2 * w + u) * 2 + (et & 1)) * 1024);
          cs[et] = mfma(a[et], ones, cs[et]);
        }
      }
#pragma unroll
      for (int et = 0; et < 4; ++et)
        if (c == 0) *(LAS f4*)(S + 114688 + w * 256 + (32 * (et >> 1) + 8 * q + 4 * (et & 1)) * 4) = cs[et];
    }
    BAR();
    if (w == 0) {
      float s = 0.f;
#pragma unroll
      for (int k = 0; k < 7; ++k) s += *(LAS float*)(S + 114688 + k * 256 + lane * 4);
      __builtin_amdgcn_raw_buffer_store_b32(asu(s), rs, lane * 4, O_CS3 + sp * 256, AUX_SC1);
    }
    VMWAIT(0);
    BAR();
    if (tid == 0) stflag(flags + F_W3 + sp, 1u);
    STAMP(2 + SPM);
    unroll<8>([&](auto g) { dmai<8192 + g * 1024>(lb, v4, d4, g * 8 * 896); });
    VMWAIT(0);
    {
      LAS char* p = S + w * 16384 + 8192 + (8 * q + pa) * 128 + 32 * pp;
      v4u a[8];
#pragma unroll
      for (int k = 0; k < 8; ++k) a[k] = trfrag(p, (k >> 2) * 4096 + 8 * (k & 3), (k >> 2) * 4096 + 512 + 8 * (k & 3));
#pragma unroll
      for (int k = 0; k < 8; ++k) {
        const int fb = 2 * sp + (k >> 2);
        STWS(a[k], l16, O_W4T + (((w * 8 + (fb & 7)) * 7 + (fb >> 3)) * 4 + (k & 3)) * 1024);
      }
    }
    VMWAIT(0);
    BAR();
    if (tid == 0) stflag(flags + F_W4 + sp, 1u);
    STAMP(3 + SPM);
    DUMP();
    return;
  }

  const int ti = w >> 1, u = w & 1, T = cl * 4 + ti;
  const int ch = w >> 2, dch = 64 * j + 2 * cl + ch;

  const v4u dx = mkdesc((const char*)x + cl * 57344), dw1 = mkdesc((const char*)w1 + j * 128), dw2 = mkdesc((const char*)w2 + j * 128);
  const u32 wk2 = wk + L_W2;
  int xb8 = j * 8192, wb8 = cq * 16384;
  u32 ee = 0, en = 0;
#define LDX() do { dma(wk2, xb8, xo, dx, xb8 >> 6); xb8 = nxt7(xb8); } while (0)
#define LDW(d, LO) do { dma((LO) ? wk2 : wk, wb8, wo, d, wb8 * 7); wb8 = nxt7(wb8); } while (0)
  if (w < 6) {
    dmai<L_XT>(wk, l16, mkdesc((const void*)parr), (64 * j + 2 * cl) * 512);
  } else {
    asm volatile("global_load_dword %0, %2, %3 sc1\n\tglobal_load_dword %1, %2, %4 sc1" : "=&v"(ee), "=&v"(en) : "v"(0), "s"(pech), "s"(pnon));
  }
  LDX(); LDW(dw1, L_W1); LDW(dw1, L_W1); LDX();
  const u32 ldw = ((const CAS u32*)ldt)[dch >> 1], dkw = ((const CAS u32*)dsk)[dch >> 1];
  u32 hoff;
  {
    const int g = ((8 * j + 2 * q + u) * 147) >> 11, sh = (g & 1) ? -1 - (g >> 1) : 1 + (g >> 1);
    const int t2 = T * 16 + c + sh, tw = t2 & 2047;
    hoff = (tw >> 4) * 14336 + q * 256 + (tw & 15) * 16 + (t2 == tw ? 0u : 0x80000000u);
  }
  const u32 xnto = (8 * w + (lane >> 3)) * 4096 + (lane & 7) * 16;
  {
    const v4u sv = {SENT, SENT, SENT, SENT};
    STWS(sv, hoff & 0x7fffffffu, O_HS + (2 * j + u) * 1024);
    STWS(sv, xnto, O_XNT + j * 262144 + cl * 128);
    STWS(sv, l16, O_X1 + (T * 14 + 2 * j + u) * 1024);
    if (q == 0) __builtin_amdgcn_raw_buffer_store_b64(v2u{SENT, SENT}, rs, c * 8, O_ST + ((T * 2 + u) * 7 + j) * 128, AUX_SC1);
#pragma unroll
    for (int s = 0; s < 4; ++s) STWS(sv, l16, O_H2 + ((cl * 4 + s) * 56 + j * 8 + w) * 1024);
  }
  u32 wa[4];
  {
    const int fsw = (pa >> 1) | ((q & 1) << 1);
    const u32 base = (16 * q + 8 * u + pa) * 128 + (((2 * pp) ^ fsw) << 4) + ((pp & 1) << 3);
#pragma unroll
    for (int p = 0; p < 4; ++p) {
      const int t = p ^ (2 * u) ^ (ti & 1);
      wa[p] = base ^ (((t >> 1) << 4) | ((t & 1) << 3));
    }
  }
  const bool sg = ((ti ^ q) & 1) != 0;
  const float dt = blo(pk(expf((dch & 1) ? bhi(ldw) : blo(ldw)), 0.f));
  const float dskip = (dch & 1) ? bhi(dkw) : blo(dkw);
  asm volatile("" : "+v"(wa[0]), "+v"(wa[1]), "+v"(wa[2]), "+v"(wa[3]), "+v"(hoff));

  f4 kp0;
  v4u KA[4];
  f2 p4, p8, g1;
  u32 non = 0;
  LAS char* const fw = S + L_C + w * FBS + lane * 4;
  LAS char* const fr = S + L_C + w * FBS + c * 288 + q * 16;
#define PUT(row, z) *(LAS u32*)(fw + (row) * 288) = pk2(z)
  {
    asm volatile("s_waitcnt vmcnt(12)" : "+v"(ee), "+v"(en)::"memory");
    if (w == 7) { non = (ee + 1 == en) ? ee + 2 : ee + 1; if (lane == 0) stflag(pnon, non); }
    BAR();
    STAMP(1);
    LAS u16* pl = (LAS u16*)(S + L_XT + ch * 512 + (64 * (w & 3) + lane) * 2);
    const float are = blo(pl[0]), aim = blo(pl[512]), bre = blo(pl[1024]), bim = blo(pl[1536]), cre = blo(pl[2048]), cim = blo(pl[2560]);
    LDX(); LDX();
    const float er = are * (dt * 1.4426950409f), ph = (dt * aim) * 0.15915494309f;
    const f2 cb = cmul(f2{cre, -cim}, f2{dt * bre, -dt * bim});
    const f2 p1 = cexp2(er, ph), p2 = cmul(p1, p1);
    p4 = cmul(p2, p2); p8 = cmul(p4, p4);
    const f2 p16 = cmul(p8, p8);
    g1 = cexp2(er * 256.f, ph * 256.f);
    SB(); LDX(); SB();
    {
      const f2 wc = {p16.x, -p16.y}, wc2 = cmul(wc, wc);
      f2 r0 = cb, r1 = cmul(cb, wc);
#pragma unroll
      for (int i = 0; i < 8; ++i) {
        PUT(2 * i, r0); PUT(2 * i + 1, r1);
        if (i < 7) { r0 = cmul(r0, wc2); r1 = cmul(r1, wc2); }
        if (i == 1 || i == 3) { SB(); LDX(); SB(); }
        if (i == 5) { SB(); LDW(dw1, L_W1); SB(); }
      }
    }
#pragma unroll
    for (int s = 0; s < 4; ++s) KA[s] = *(LAS v4u*)(fr + s * 64);
    CFENCE(); LDW(dw1, L_W1); SB();
    {
      f2 z = p1, y = p2;
#pragma unroll
      for (int i = 0; i < 4; ++i) {
        PUT(2 * i, z); PUT(2 * i + 1, cmul(z, p2)); PUT(8 + i, y); PUT(12 + i, cmul(y, g1));
        if (i < 3) { z = cmul(z, p4); y = cmul(y, p4); SB(); LDW(dw1, L_W1); SB(); }
      }
    }
    kp0 = f4{0, 0, 0, 0};
#pragma unroll
    for (int s = 0; s < 4; ++s) kp0 = mfma(KA[s], *(LAS v4u*)(fr + s * 64), kp0);
    CFENCE();
  }
  STAMP(2);

  v4u xpc, xnp;
  {
    VMWAIT(5);
    BAR();
    STAMP(3);
    v4u X[7];
    {
      LAS char* xr = S + L_W2 + (16 * ti + c) * 128 + (((2 * q + u) ^ (((c >> 1) + 6) & 7)) << 4);
#pragma unroll
      for (int b = 0; b < 7; ++b) X[b] = *(LAS v4u*)(xr + b * 8192);
      xpc = *(LAS v4u*)(xr + j * 8192);
    }
    f4 G = {0, 0, 0, 0}, Sx = {0, 0, 0, 0};
#pragma unroll
    for (int b = 0; b < 7; ++b) { G = mfma(X[b], X[b], G); Sx = mfma(ones, X[b], Sx); }
    const float gd = (c & 2) ? ((c & 1) ? G[3] : G[2]) : ((c & 1) ? G[1] : G[0]);
    if (q == (c >> 2)) *(LAS f2*)(S + L_ST + (ti * 16 + c) * 16 + u * 8) = f2{Sx[0], gd};
    VMWAIT(0);
    BAR();
    STAMP(4);
    if (w == 7) asm volatile("global_load_dword %0, %1, %2 sc1" : "=&v"(ee) : "v"(0), "s"(pech));
    f4 acc[4] = {{0, 0, 0, 0}, {0, 0, 0, 0}, {0, 0, 0, 0}, {0, 0, 0, 0}}, cs = {0, 0, 0, 0};
    const bool hi2 = (ti & 2) != 0;
    v4u A[2][4];
#pragma unroll
    for (int p = 0; p < 4; ++p) A[0][p] = trfrag(S + L_W1 + wa[p], 0, 512);
    SB();
#pragma unroll
    for (int b = 0; b < 7; ++b) {
      LDW(dw2, L_W2);
#pragma unroll
      for (int p4_ = 0; p4_ < 4; ++p4_) {
        const int p = (p4_ + 2) & 3;
        acc[p] = mfma(A[b & 1][p], X[b], acc[p]);
        if (b < 6) A[(b + 1) & 1][p] = trfrag(S + L_W1 + wa[p], (b + 1) * 8192, (b + 1) * 8192 + 512);
        SB();
      }
      const v4u s0 = A[b & 1][0], s2 = A[b & 1][2];
      cs = mfma(v4u{hi2 ? s2.x : s0.x, hi2 ? s2.y : s0.y, hi2 ? s2.z : s0.z, hi2 ? s2.w : s0.w}, ones, cs);
      SB();
    }
    *(LAS f4*)(S + L_C + w * FBS + l16) = acc[2];
    *(LAS f4*)(S + L_C + w * FBS + 1024 + l16) = acc[3];
    if (c == 0) *(LAS f4*)(S + L_CS + u * 256 + ((ti & 2) ^ (2 * u) ^ (ti & 1)) * 64 + q * 16) = cs;
    if (w == 7) {
      asm volatile("s_waitcnt vmcnt(7)" : "+v"(ee)::"memory");
NOUNROLL
      for (int spin = 0; spin < SPIN && !__all(ee == non); ++spin) { RETRY(0); ee = ldflag(pech); }
      if (lane == 0) { stflag(flags + F_CLR + cl * 8 + j, 1u); stflag(flags + F_ACK + blockIdx.x, 1u); }
    }
    BAR();
    STAMP(5);
    const f4 st = *(LAS f4*)(S + L_ST + (ti * 16 + c) * 16);
    const f4 o0 = acc[0] + *(LAS f4*)(S + L_C + (w ^ 1) * FBS + l16), o1 = acc[1] + *(LAS f4*)(S + L_C + (w ^ 1) * FBS + 1024 + l16);
    LAS char* cp = S + L_CS + (2 * u + (ti & 1)) * 64 + q * 16;
    LAS char* cp1 = S + L_CS + (2 * u + 1 - (ti & 1)) * 64 + q * 16;
    const f4 c0 = *(LAS f4*)(cp) + *(LAS f4*)(cp + 256), c1 = *(LAS f4*)(cp1) + *(LAS f4*)(cp1 + 256);
    const float mean = (st[0] + st[2]) * (1.f / 448.f);
    const float var = __builtin_fmaxf(__builtin_fmaf(st[1] + st[3], 1.f / 448.f, -mean * mean), 0.f);
    const float r1 = __builtin_amdgcn_rsqf(var + 1e-5f);
    const float rs2 = r1 * __builtin_amdgcn_rsqf(__builtin_fmaf(var * r1, r1, 1e-5f));
    const f2 vm = sp2(-mean), vr = sp2(rs2);
    const u32 a0 = gelu_pk(fma2(c0.xy, vm, o0.xy) * vr), a1 = gelu_pk(fma2(c0.zw, vm, o0.zw) * vr);
    const u32 b0 = gelu_pk(fma2(c1.xy, vm, o1.xy) * vr), b1 = gelu_pk(fma2(c1.zw, vm, o1.zw) * vr);
    v4u hv = {sg ? b0 : a0, sg ? b1 : a1, sg ? a0 : b0, sg ? a1 : b1};
    if ((int)hoff < 0) hv = v4u{0, 0, 0, 0};
    STWS(hv, hoff & 0x7fffffffu, O_HS + (2 * j + u) * 1024);
    STAMP(6);
    const f2 v1 = sp2(r1), vn = sp2(-mean * r1);
#pragma unroll
    for (int k = 0; k < 4; ++k) xnp[k] = pk2(fma2(unpk(xpc[k]), v1, vn));
  }

  v4u W3[28], H[7];
  u32 fl2;
  GAS u32* const pf2 = flags + F_CLR + (lane & 31) * 8 + j;
  const u32 w3b = O_W3T + (8 * j + w) * 28672, hsb = O_HS + (T * 14 + u) * 1024;
#define LDW3(k) W3[k] = LDWS(l16 + ((k) & 3) * 1024, w3b + ((k) >> 2) * 4096)
#define LDHS() _Pragma("unroll") for (int b = 0; b < 7; ++b) H[b] = LDWS(l16 + (b & 1) * 2048, hsb + (b >> 1) * 4096)
  const int tp = tid & 255;
  {
    LAS char* xt = S + L_XT + (16 * q + 8 * u) * 144 + (16 * ti + c) * 2;
#pragma unroll
    for (int k = 0; k < 4; ++k) {
      *(LAS u16*)(xt + (2 * k) * 144) = (u16)xnp[k];
      *(LAS u16*)(xt + (2 * k + 1) * 144) = (u16)(xnp[k] >> 16);
    }
    BAR();
    STWS(*(LAS v4u*)(S + L_XT + (8 * w + (lane >> 3)) * 144 + (lane & 7) * 16), xnto, O_XNT + j * 262144 + cl * 128);
    STAMP(7);
    const f2 g2 = cmul(g1, g1), g3 = cmul(g2, g1), p12 = cmul(p4, p8);
    PUT(0, p4); PUT(1, p12); PUT(2, cmul(p4, g1)); PUT(3, cmul(p12, g1)); PUT(4, cmul(p4, g2)); PUT(5, cmul(p12, g2));
    PUT(6, cmul(p4, g3)); PUT(7, cmul(p12, g3));
    PUT(8, (f2{1.f, 0.f})); PUT(9, p8); PUT(10, g1); PUT(11, cmul(p8, g1)); PUT(12, g2); PUT(13, cmul(p8, g2)); PUT(14, g3); PUT(15, cmul(p8, g3));
    v4u B[4];
#pragma unroll
    for (int s = 0; s < 4; ++s) B[s] = *(LAS v4u*)(fr + s * 64);
    CFENCE();
    GAS u32* const pf1 = lane < 24 ? flags + F_CLR + ((cl + (lane >> 3) - 1) & 31) * 8 + min(lane & 7, 6) : flags + F_W3 + 4 * j + ti;
    const u32 fl1 = ldflag(pf1);
    SB();
    {
      f2 z = cmul(g2, g2);
#pragma unroll
      for (int i = 0; i < 4; ++i) { PUT(2 * i, z); PUT(2 * i + 1, cmul(z, p8)); z = cmul(z, g1); }
      PUT(8, z);
    }
    f4 kp1 = {0, 0, 0, 0}, kp2 = {0, 0, 0, 0};
#pragma unroll
    for (int s = 0; s < 4; ++s) kp1 = mfma(KA[s], B[s], kp1);
#pragma unroll
    for (int s = 0; s < 4; ++s) kp2 = mfma(KA[s], *(LAS v4u*)(fr + s * 64), kp2);
    CFENCE();
    LAS char* pw = S + L_C + w * FBS + c * 64 + q * 16;
    *(LAS f4*)(pw) = kp0; *(LAS f4*)(pw + 1024) = kp1; *(LAS f4*)(pw + 2048) = kp2;
    *(LAS v4u*)(S + L_C + w * FBS + 3072 + l16) = xpc;
    if (tid < 32) *(LAS v4u*)(S + L_KC + (tid >> 2) * KCS + 4096 + (tid & 3) * 16) = v4u{0, 0, 0, 0};
    else if (tid < 160) *(LAS v4u*)(S + L_U + ((tid - 32) >> 4) * 1280 + (tid & 15) * 16) = v4u{0, 0, 0, 0};
    else if (tid < 162) *(LAS v2u*)(S + L_KT + (tid - 160) * KTS) = v2u{0, 0};
    STAMP(8);
    wait_flags(pf1, fl1);
    VMWAIT(0);
    LDHS();
    fl2 = ldflag(pf2);
    SB();
    STAMP(9);
    BAR();
    if (tp < 164) {
      LAS char* pp4 = S + L_C + ch * 4 * FBS + tp * 16;
      const f4 v = (*(LAS f4*)(pp4) + *(LAS f4*)(pp4 + FBS)) + (*(LAS f4*)(pp4 + 2 * FBS) + *(LAS f4*)(pp4 + 3 * FBS));
      const int col = tp >> 2, k8 = col & 7;
      const int cv = col < 8 ? 2 * col + 1 : col < 16 ? 2 + 4 * (col & 3) + 64 * (k8 & 4) : 8 * (col & 1) + (col < 24 ? 4 + 128 * (k8 & 6) : 128 * ((col - 24) & 30));
      LAS char* kt = S + L_KT + ch * KTS + 8 + cv * 2 + (tp & 3) * 128;
      const u32 k01 = pk(v[0], v[1]), k23 = pk(v[2], v[3]);
      if (tp <= 160) *(LAS u16*)(kt) = (u16)k01;
      if (tp < 160) { *(LAS u16*)(kt + 32) = (u16)(k01 >> 16); *(LAS u16*)(kt + 64) = (u16)k23; *(LAS u16*)(kt + 96) = (u16)(k23 >> 16); }
    }
    SB(); LDW3(0); LDW3(1); SB();
    int ga[11];
    {
      const float m0 = (float)(2047 - 8 * tp);
#pragma unroll
      for (int e = 0; e < 12; e += 2) {
        const u32 r = pk(m0 - (float)e, m0 - (float)(e + 1));
        ga[e] = 2 * (int)blo(r);
        if (e < 10) ga[e + 1] = 2 * (int)bhi(r);
      }
    }
    BAR();
    {
      u32 v[11], pr[10];
#pragma unroll
      for (int e = 0; e < 11; ++e) v[e] = *(LAS u16*)(S + L_KT + 8 + ch * KTS + ga[e]);
      SB(); LDW3(2); LDW3(3); SB();
#pragma unroll
      for (int e = 0; e < 10; ++e) pr[e] = v[e] | (v[e + 1] << 16);
#pragma unroll
      for (int k = 0; k < 4; ++k) *(LAS v4u*)(S + L_KC + (ch * 4 + k) * KCS + tp * 16) = v4u{pr[3 - k], pr[5 - k], pr[7 - k], pr[9 - k]};
    }
    STAMP(10);
  }

  f4 cs3a, cs3b;
  {
    v4u A[2][4];
#pragma unroll
    for (int p = 0; p < 4; ++p) A[0][p] = trfrag(S + L_W2 + wa[p], 0, 512);
    SB();
    wait_flags(pf2, fl2);
    v4u uv = LDWS(tp * 16, O_XNT + dch * 4096);
    SB();
    f4 acc[4] = {{0, 0, 0, 0}, {0, 0, 0, 0}, {0, 0, 0, 0}, {0, 0, 0, 0}};
#pragma unroll
    for (int b = 0; b < 7; ++b) {
#pragma unroll
      for (int p4_ = 0; p4_ < 4; ++p4_) {
        const int p = (p4_ + 2) & 3;
        acc[p] = mfma(A[b & 1][p], H[b], acc[p]);
        if (b < 6) A[(b + 1) & 1][p] = trfrag(S + L_W2 + wa[p], (b + 1) * 8192, (b + 1) * 8192 + 512);
        if (p4_ == 3) LDW3(4 + b);
        SB();
      }
    }
    if (__any(max3u(max3u(H[0].x, H[1].x, H[2].x), max3u(H[3].x, H[4].x, H[5].x), H[6].x) == SENT)) {
NOUNROLL
      for (int spin = 0; spin < SPIN; ++spin) {
        RETRY(3);
        CFENCE();
        LDHS();
        if (!__any(max3u(max3u(H[0].x, H[1].x, H[2].x), max3u(H[3].x, H[4].x, H[5].x), H[6].x) == SENT)) break;
      }
#pragma unroll
      for (int p = 0; p < 4; ++p) {
        acc[p] = f4{0, 0, 0, 0};
#pragma unroll
        for (int b = 0; b < 7; ++b) acc[p] = mfma(trfrag(S + L_W2 + wa[p], b * 8192, b * 8192 + 512), H[b], acc[p]);
      }
    }
    *(LAS f4*)(S + L_C + w * FBS + l16) = acc[2];
    *(LAS f4*)(S + L_C + w * FBS + 1024 + l16) = acc[3];
NOUNROLL
    for (int spin = 0; spin < SPIN && __any(uv.x == SENT); ++spin) { RETRY(4); CFENCE(); uv = LDWS(tp * 16, O_XNT + dch * 4096); }
    *(LAS v4u*)(S + L_U + ch * 5120 + (tp & 3) * 1280 + 256 + (tp >> 2) * 16) = uv;
    BAR();
    STAMP(11);
    const f4 o0 = acc[0] + *(LAS f4*)(S + L_C + (w ^ 1) * FBS + l16), o1 = acc[1] + *(LAS f4*)(S + L_C + (w ^ 1) * FBS + 1024 + l16);
    const u32 n0 = sg ? xnp[2] : xnp[0], n1 = sg ? xnp[3] : xnp[1], n2 = sg ? xnp[0] : xnp[2], n3 = sg ? xnp[1] : xnp[3];
    const u32 a0 = pk2(o0.xy + unpk(n0)), a1 = pk2(o0.zw + unpk(n1)), b0 = pk2(o1.xy + unpk(n2)), b1 = pk2(o1.zw + unpk(n3));
    const v4u x1p = v4u{sg ? b0 : a0, sg ? b1 : a1, sg ? a0 : b0, sg ? a1 : b1};
    STWS(x1p, l16, O_X1 + (T * 14 + 2 * j + u) * 1024);
    *(LAS v4u*)(S + L_C + w * FBS + 2048 + l16) = x1p;
    const f2 e0 = unpk(a0), e1 = unpk(a1), e2 = unpk(b0), e3 = unpk(b1);
    const f2 t1 = (e0 + e1) + (e2 + e3), t2 = fma2(e0, e0, fma2(e1, e1, fma2(e2, e2, e3 * e3)));
    float s1 = t1.x + t1.y, s2 = t2.x + t2.y;
    s1 += __shfl_xor(s1, 16); s2 += __shfl_xor(s2, 16);
    s1 += __shfl_xor(s1, 32); s2 += __shfl_xor(s2, 32);
    if (q == 0) __builtin_amdgcn_raw_buffer_store_b64(v2u{asu(s1), asu(s2)}, rs, c * 8, O_ST + ((T * 2 + u) * 7 + j) * 128, AUX_SC1);
    SB(); LDW3(11); SB();
    STAMP(12);
  }

  GAS u32* const pf4 = flags + F_W4 + 4 * min(lane & 7, 6) + ti;
  u32 fl4;
  {
    const int rho = (w >> 1) & 1;
    v2u sa, sb;
    const v4u dws = mkdesc(ws);
    const u32 x1b = O_X1 + (T * 14 + u) * 1024, x1l = L_W2 + (ti * 14 + u) * 1024;
#define LDX1() do { \
      unroll<7>([&](auto b) { dmai_sc1<b * 2048>(sb0 + x1l, l16, dws, x1b + b * 2048); }); \
      sa = __builtin_amdgcn_raw_buffer_load_b64(rs, q * 128 + c * 8, O_ST + (T * 2 + u) * 896, AUX_SC1); \
      sb = __builtin_amdgcn_raw_buffer_load_b64(rs, min(q + 4, 6) * 128 + c * 8, O_ST + (T * 2 + u) * 896, AUX_SC1); } while (0)
    LAS char* pa_ = S + L_KC + (ch * 4 + (c & 3)) * KCS + 2 * (2044 - (c & 12) + 8 * q - 16 * rho - 32 * u) - 3072 - 896;
    LAS char* pb_ = S + L_U + ch * 5120 + q * 1280 + (16 + c - u) * 16 - 224;
    f4 y[4] = {{0, 0, 0, 0}, {0, 0, 0, 0}, {0, 0, 0, 0}, {0, 0, 0, 0}};
    v4u B[2][4], A[2][4];
#define CLDB(it, a) B[(it) & 1][a] = *(LAS v4u*)(pb_ + (7 - (it)) * 32 + (a) * 256)
#define CLDA(it, a) do { const v2u lo = *(volatile LAS v2u*)(pa_ + (7 - (it)) * 128 + (3 - (a)) * 1024), hi = *(volatile LAS v2u*)(pa_ + (7 - (it)) * 128 + (3 - (a)) * 1024 + 8); \
      A[(it) & 1][a] = v4u{lo.x, lo.y, hi.x, hi.y}; } while (0)
#pragma unroll
    for (int a = 0; a < 4; ++a) { CLDB(0, a); CLDA(0, a); }
    SB();
#pragma unroll
    for (int it = 0; it < 8; ++it) {
      int n = 0;
#pragma unroll
      for (int a = 0; a < 4; ++a)
#pragma unroll
        for (int k = a; k < 4; ++k) {
          y[k] = mfma(A[it & 1][a], B[it & 1][k - a], y[k]);
          if (it < 7 && n < 8) { if (n & 1) CLDB(it + 1, n >> 1); else CLDA(it + 1, n >> 1); }
          if (n == 8) LDW3(12 + 2 * it);
          if (n == 9) LDW3(13 + 2 * it);
          SB();
          ++n;
        }
      if (it == XIT) { LDX1(); SB(); }
    }
    cs3a = __builtin_bit_cast(f4, LDWS(q * 32, O_CS3 + (8 * j + w) * 128));
    cs3b = __builtin_bit_cast(f4, LDWS(q * 32 + 16, O_CS3 + (8 * j + w) * 128));
    fl4 = ldflag(pf4);
    SB();
    STAMP(13);
    const f4 e0 = u ? y[0] : y[2], e1 = u ? y[1] : y[3], k0 = u ? y[2] : y[0], k1 = u ? y[3] : y[1];
    *(LAS f4*)(S + L_C + w * FBS + l16) = e0;
    *(LAS f4*)(S + L_C + w * FBS + 1024 + l16) = e1;
    BAR();
#pragma unroll
    for (int k = 0; k < 2; ++k) {
      const f4 v = (k ? k1 : k0) + *(LAS f4*)(S + L_C + (w ^ 1) * FBS + k * 1024 + l16);
      const int blk = 32 * u + c;
      const v2u uu = *(volatile LAS v2u*)(S + L_U + ch * 5120 + (2 * rho + (q >> 1)) * 1280 + (16 + blk) * 16 + (q & 1) * 8 + k * 256);
      *(LAS v2u*)(S + L_KT + ch * 4096 + blk * 64 + rho * 32 + q * 8 + k * 1024) =
          v2u{pk2(fma2(unpk(uu.x), sp2(dskip), v.xy)), pk2(fma2(unpk(uu.y), sp2(dskip), v.zw))};
    }
    STAMP(14);
NOUNROLL
    for (int spin = 0; spin < SPIN; ++spin) {
      asm volatile("s_waitcnt vmcnt(0)" : "+v"(sa), "+v"(sb)::"memory");
      u32 m = max(sa.x, sb.x);
#pragma unroll
      for (int b = 0; b < 7; ++b) m = max(m, *(LAS u32*)(S + x1l + b * 2048 + l16));
      if (!__any(m == SENT)) break;
      RETRY(5);
      LDX1();
    }
    {
      float s1 = asf(sa.x) + (q < 3 ? asf(sb.x) : 0.f), s2 = asf(sa.y) + (q < 3 ? asf(sb.y) : 0.f);
      s1 += __shfl_xor(s1, 16); s2 += __shfl_xor(s2, 16);
      s1 += __shfl_xor(s1, 32); s2 += __shfl_xor(s2, 32);
      if (q == 0) *(LAS f2*)(S + L_ST + (ti * 16 + c) * 16 + u * 8) = f2{s1, s2};
    }
    STAMP(15);
    BAR();
    STWS(*(LAS v4u*)(S + L_KT + tid * 16), tp * 16, O_ST2 + dch * 4096);
    wait_flags(pf4, fl4);
    STAMP(16);
  }

  v4u W4[28], own[4], H2[4][6];
  int bo[7];
  bo[0] = j * 8192;
#pragma unroll
  for (int b = 1; b < 7; ++b) bo[b] = nxt7(bo[b - 1]);
  const u32 w4b = O_W4T + (j * 8 + w) * 28672, h2b = O_H2 + cl * 229376 + w * 1024;
#define LDW4(k) W4[k] = LDWS(l16 + ((k) & 3) * 1024, w4b + (bo[(k) >> 2] >> 1))
#define LDH2(s) _Pragma("unroll") for (int b = 1; b < 7; ++b) H2[s][b - 1] = LDWS(l16, h2b + ((ti + 1 + (s)) & 3) * 57344 + bo[b])
#define CKH2(s) NOUNROLL for (int spin = 0; spin < SPIN && __any(max3u(max3u(H2[s][0].x, H2[s][1].x, H2[s][2].x), max3u(H2[s][3].x, H2[s][4].x, H2[s][5].x), 0u) == SENT); ++spin) { \
    RETRY(6); CFENCE(); LDH2(s); }
  if (w < 4) __builtin_amdgcn_s_setprio(1);
  v4u Bq[4];
#pragma unroll
  for (int k = 0; k < 3; ++k) Bq[k] = *(LAS v4u*)(S + L_W2 + ((ti + 1) & 3) * 14336 + l16 + k * 1024);
#pragma unroll
  for (int s = 0; s < 4; ++s) {
    const int tl = (ti + 1 + s) & 3;
    LAS char* ap = S + L_W2 + tl * 14336 + l16;
    LAS char* an = S + L_W2 + ((tl + 1) & 3) * 14336 + l16;
    const f4 st = *(LAS f4*)(S + L_ST + (tl * 16 + c) * 16);
    f4 a0 = {0, 0, 0, 0}, a1 = {0, 0, 0, 0};
#pragma unroll
    for (int ks = 0; ks < 14; ++ks) {
      const int qi = (s * 14 + ks) & 3, qn = (s * 14 + ks + 3) & 3;
      a0 = mfma(W3[2 * ks], Bq[qi], a0);
      if (ks + 3 < 14) Bq[qn] = *(LAS v4u*)(ap + (ks + 3) * 1024);
      else if (s < 3) Bq[qn] = *(LAS v4u*)(an + (ks - 11) * 1024);
      SB();
      a1 = mfma(W3[2 * ks + 1], Bq[qi], a1);
      if (s < 3 && ks % 3 == 0 && s * 5 + ks / 3 < 14) LDW4(s * 5 + ks / 3);
      if (s == 3) LDW4(14 + ks);
      SB();
    }
    if (s == 0) {
      VMWAIT(5);
      if (lane == 0) stflag(flags + F_S + j * 256 + (w & 3) * 64 + cl * 2 + ch, 1u);
    }
    const float mean = (st[0] + st[2]) * (1.f / 448.f);
    const f2 vr = sp2(__builtin_amdgcn_rsqf(__builtin_fmaxf(__builtin_fmaf(st[1] + st[3], 1.f / 448.f, -mean * mean), 0.f) + 1e-5f)), vm = sp2(-mean);
    own[s] = v4u{gelu_pk(fma2(cs3a.xy, vm, a0.xy) * vr), gelu_pk(fma2(cs3a.zw, vm, a0.zw) * vr),
                 gelu_pk(fma2(cs3b.xy, vm, a1.xy) * vr), gelu_pk(fma2(cs3b.zw, vm, a1.zw) * vr)};
    STWS(own[s], l16, h2b + tl * 57344 + bo[0]);
    SB();
    if (s >= 2) { LDH2(s - 2); SB(); }
    STAMP(17 + s);
  }
  GAS u32* const pfs = flags + F_S + j * 256 + (cl >> 3) * 64 + lane;
  const u32 fls = ldflag(pfs);
  f4 o[4][4];
#pragma unroll
  for (int s = 0; s < 4; ++s)
#pragma unroll
    for (int t = 0; t < 4; ++t) o[s][t] = mfma(W4[t], own[s], f4{0, 0, 0, 0});
  SB();
  STAMP(21);
  f4 pre0 = {0, 0, 0, 0}, pre1 = {0, 0, 0, 0};
#pragma unroll
  for (int s = 0; s < 4; ++s) {
    const int tl = (ti + 1 + s) & 3;
    CKH2(s);
#pragma unroll
    for (int b = 1; b < 7; ++b) {
      f4 r0, r1;
      if (s == 3) {
        r0 = *(LAS f4*)(S + (ti * 7 + b - 1) * 4096 + u * 2048 + l16);
        r1 = *(LAS f4*)(S + (ti * 7 + b - 1) * 4096 + u * 2048 + 1024 + l16);
      }
#pragma unroll
      for (int t = 0; t < 4; ++t) o[s][t] = mfma(W4[4 * b + t], H2[s][b - 1], o[s][t]);
      if (s == 3) { pre0 += r0; pre1 += r1; }
      SB();
    }
    if (s < 2) { LDH2(s + 2); SB(); }
    if (s == 0) {
      wait_flags(pfs, fls);
      dmai_sc1<L_XT>(wk, (lane >> 3) * 4096 + (lane & 7) * 16, mkdesc(ws), O_ST2 + (64 * j + 8 * w) * 4096 + cl * 128);
      BAR();
    }
    if (s < 3) {
#pragma unroll
      for (int t = 0; t < 4; ++t) *(LAS f4*)(S + (tl * 7 + 2 * s + u) * 4096 + t * 1024 + l16) = o[s][t];
    }
    if (s == 2) { VMWAIT(6); BAR(); }
    STAMP(22 + s);
  }
  {
    const f4 e0 = u ? o[3][0] : o[3][2], e1 = u ? o[3][1] : o[3][3], k0 = u ? o[3][2] : o[3][0], k1 = u ? o[3][3] : o[3][1];
    *(LAS f4*)(S + (ti * 7 + 6) * 4096 + (u ^ 1) * 2048 + l16) = e0;
    *(LAS f4*)(S + (ti * 7 + 6) * 4096 + (u ^ 1) * 2048 + 1024 + l16) = e1;
    const v4u xq = *(LAS v4u*)(S + L_C + w * FBS + 3072 + l16), x1p = *(LAS v4u*)(S + L_C + w * FBS + 2048 + l16);
    u32 lz = l16;
    asm volatile("" : "+v"(lz));
    const v4u sp_ = trfrag(S + L_XT + u * 1024 + ti * 32 + (lz >> 8) * 2048 + ((lz >> 6) & 3) * 128 + ((lz >> 4) & 3) * 8, 0, 512);
    const f4 q0 = k0 + pre0, q1 = k1 + pre1;
    BAR();
    const f4 v0 = q0 + *(LAS f4*)(S + (ti * 7 + 6) * 4096 + u * 2048 + l16), v1 = q1 + *(LAS f4*)(S + (ti * 7 + 6) * 4096 + u * 2048 + 1024 + l16);
    v4u r;
#pragma unroll
    for (int k = 0; k < 4; ++k) {
      const f2 v = (k == 0 ? v0.xy : k == 1 ? v0.zw : k == 2 ? v1.xy : v1.zw) + unpk(x1p[k]);
      r[k] = pk2(fma2(unpk(sp_[k]), v * sigm2(v * sp2(-1.4426950409f)), unpk(xq[k])));
    }
    __builtin_amdgcn_raw_buffer_store_b128(r, __builtin_amdgcn_make_buffer_rsrc(out, 0, 1835008, 0x00020000), ((lz >> 4) & 15) * 896 + (lz >> 8) * 32, T * 14336 + 128 * j + 16 * u, AUX_SC1);
    STAMP(26);
    DUMP();
    while (__builtin_amdgcn_s_memrealtime() < tstart_ + 6000 + 4 * (tev_ - tstart_)) __builtin_amdgcn_s_sleep(2);
  }
}

extern "C" void kernel_launch(void* const* d_in, const int* in_sizes, int n_in, void* d_out, int out_size, void* d_ws, size_t ws_size,
                              hipStream_t stream) {
  fused_kernel<<<256, 512, 0, stream>>>((const u16*)d_in[0], (const u16*)d_in[1], (const u16*)d_in[2], (const u16*)d_in[3], (const u16*)d_in[4],
                                        (const u16*)d_in[5], (const u16*)d_in[6], (const u16*)d_in[7], (const u16*)d_in[8], (const u16*)d_in[10],
                                        (const u16*)d_in[11], (const u16*)d_in[12], (const u16*)d_in[13], (u16*)d_out, (unsigned char*)d_ws);
}
```

```cpp
#include <hip/hip_runtime.h>
#include <type_traits>
typedef unsigned short u16;
typedef unsigned u32;
typedef unsigned long long u64;
typedef u32 v4u __attribute__((ext_vector_type(4)));
typedef u32 v2u __attribute__((ext_vector_type(2)));
typedef float f4 __attribute__((ext_vector_type(4)));
typedef float f2 __attribute__((ext_vector_type(2)));
typedef short s8v __attribute__((ext_vector_type(8)));
typedef short s4v __attribute__((ext_vector_type(4)));
typedef __bf16 bf2 __attribute__((ext_vector_type(2)));
#define LAS __attribute__((address_space(3)))
#define GAS __attribute__((address_space(1)))
#define CAS __attribute__((address_space(4)))
#define RLX_AGENT __ATOMIC_RELAXED, __HIP_MEMORY_SCOPE_AGENT
#define DEV __device__ __forceinline__
#define SB() __builtin_amdgcn_sched_barrier(0)
#define CFENCE() asm volatile("" ::: "memory")
#define VMWAIT(n) asm volatile("s_waitcnt vmcnt(" #n ")" ::: "memory")
#define BAR() asm volatile("s_waitcnt lgkmcnt(0)\n\ts_barrier" ::: "memory")
#define LGKM(n) __builtin_amdgcn_s_waitcnt(0xC07F | ((n) << 8))
#define RBAR() asm volatile("s_barrier" ::: "memory")
#define NOUNROLL _Pragma("clang loop unroll(disable)")

constexpr u32 F_CLR = 0, F_XT = 1024, F_W3 = 1920, F_W4 = 1952, F_Z2 = 1984, F_ACK = 2048, F_S = 3072, F_C2 = 4864, F_C1 = 6912;
constexpr u32 O_ECH = 65536;
constexpr u32 O_HS = 131072, O_X1 = O_HS + 1835008, O_ST = O_X1 + 1835008, O_H2 = O_ST + 229376, O_XNT = O_H2 + 7340032,
              O_ST2 = O_XNT + 1835008, O_W3T = O_ST2 + 1835008, O_W4T = O_W3T + 1605632, O_CS3 = O_W4T + 1605632,
              O_DBG = O_CS3 + 8192, WS_END = O_DBG + (1 << 20);
constexpr u32 L_C = 0, FBS = 8704, L_X1S = 0, L_SR = 8192, L_SB = 16384;
constexpr u32 L_XS = 34816, L_ST = 43008, L_CS = 44032, L_CNT = 44288, L_W1 = 49152, L_W2 = 106496;
constexpr u32 L_KC = L_W1, KCS = 4160, L_U = L_W1 + 33280, L_XT = L_U, L_KT = L_W1 + 43520, KTS = 4128;
constexpr int AUX_SC1 = 16, SPIN = 1 << 15;
constexpr u32 SENT = 0xffffffffu;
constexpr int CODEL = 76;
#define FD0 9
#define W4DLY 40
#define NB3 10
#define NB3B 14
#define NB3C 4
#define NA3 8
#define NA3B 8
#define P3B 2
#define NCB 0
#define XDLY 0

#define SPM 0
#define STAMP(k)
#define CSTAMP(k)
#define DUMP()
#define RETRY(k)

#define APM 0
#define FS(k)
#define LS(k) STAMP(k)

DEV f4 mfma(v4u a, v4u b, f4 c) { return __builtin_amdgcn_mfma_f32_16x16x32_bf16(__builtin_bit_cast(s8v, a), __builtin_bit_cast(s8v, b), c, 0, 0, 0); }
DEV u32 pk2(f2 a) { return __builtin_bit_cast(u32, __builtin_convertvector(a, bf2)); }
DEV u32 pk(float a, float b) { return pk2(f2{a, b}); }
DEV float asf(u32 v) { return __builtin_bit_cast(float, v); }
DEV u32 asu(float v) { return __builtin_bit_cast(u32, v); }
DEV float blo(u32 v) { return asf(v << 16); }
DEV float bhi(u32 v) { return asf(v & 0xffff0000u); }
DEV f2 unpk(u32 v) { return f2{blo(v), bhi(v)}; }
DEV f2 sp2(float v) { return f2{v, v}; }
DEV f2 fma2(f2 a, f2 b, f2 c) { return __builtin_elementwise_fma(a, b, c); }
DEV f2 sigm2(f2 t) {
  const f2 d = f2{__builtin_amdgcn_exp2f(t.x), __builtin_amdgcn_exp2f(t.y)} + sp2(1.0f);
  return f2{__builtin_amdgcn_rcpf(d.x), __builtin_amdgcn_rcpf(d.y)};
}
DEV u32 gelu_pk(f2 z) { return pk2(z * sigm2(z * fma2(z * z, sp2(-0.1029432397f), sp2(-2.302208198f)))); }
DEV u32 pks(float a, float b) { u32 r; asm("v_cvt_pk_bf16_f32 %0, %1, %2" : "=v"(r) : "v"(a), "v"(b)); return r; }
DEV float adds(float a, float b) { float r; asm("v_add_f32 %0, %1, %2" : "=v"(r) : "v"(a), "v"(b)); return r; }
DEV float gelu_s(float acc, float cs, f4 st) {
  const float a = __builtin_fmaf(cs, st.x, acc);
  const float e = __builtin_amdgcn_exp2f(a * __builtin_fmaf(a * a, st.z, st.y));
  return a * __builtin_amdgcn_rcpf(__builtin_fmaf(e, st.w, st.w));
}
DEV v2u trd(LAS char* p) { return __builtin_bit_cast(v2u, __builtin_amdgcn_ds_read_tr16_b64_v4i16((LAS s4v*)p)); }
DEV v4u trfrag(LAS char* p, int o0, int o1) { const v2u a = trd(p + o0), b = trd(p + o1); return v4u{a.x, a.y, b.x, b.y}; }
DEV v4u mkdesc(const void* p) { const u64 a = (u64)p; return v4u{(u32)a, (u32)(a >> 32) & 0xffffu, 0x7fffffffu, 0x20000u}; }
DEV void dma(u32 la, u32 lb, u32 voff, v4u d, u32 soff) {
  asm volatile("s_add_u32 m0, %0, %1\n\ts_nop 0\n\tbuffer_load_dwordx4 %2, %3, %4 offen lds" ::"s"(la), "s"(lb), "v"(voff), "s"(d), "s"(soff) : "scc");
}
DEV void dma_sc1(u32 la, u32 lb, u32 voff, v4u d, u32 soff) {
  asm volatile("s_add_u32 m0, %0, %1\n\ts_nop 0\n\tbuffer_load_dwordx4 %2, %3, %4 offen sc1 lds" ::"s"(la), "s"(lb), "v"(voff), "s"(d), "s"(soff) : "scc");
}
template <int LB> DEV void dmai(u32 la, u32 voff, v4u d, u32 soff) {
  asm volatile("s_add_u32 m0, %0, %1\n\ts_nop 0\n\tbuffer_load_dwordx4 %2, %3, %4 offen lds" ::"s"(la), "i"(LB), "v"(voff), "s"(d), "s"(soff) : "scc");
}
template <int LB> DEV void dmai_sc1(u32 la, u32 voff, v4u d, u32 soff) {
  asm volatile("s_add_u32 m0, %0, %1\n\ts_nop 0\n\tbuffer_load_dwordx4 %2, %3, %4 offen sc1 lds" ::"s"(la), "i"(LB), "v"(voff), "s"(d), "s"(soff) : "scc");
}
template <int N, class F> DEV void unroll(F f) { if constexpr (N > 0) { unroll<N - 1>(f); f(std::integral_constant<int, N - 1>{}); } }
DEV u32 ldflag(GAS u32* p) { return __hip_atomic_load(p, RLX_AGENT); }
DEV void stflag(GAS u32* p, u32 v) { __hip_atomic_store(p, v, RLX_AGENT); }
DEV void wait_flags(GAS u32* p, u32 v) {
  if (!__all(v != 0u)) {
NOUNROLL
    for (int spin = 0; spin < SPIN && !__all(ldflag(p) != 0u); ++spin) __builtin_amdgcn_s_sleep(4);
  }
  __builtin_amdgcn_fence(__ATOMIC_ACQUIRE, "wavefront");
}
DEV u32 max3u(u32 a, u32 b, u32 c) { return max(max(a, b), c); }
DEV f2 cmul(f2 a, f2 b) {
  f2 r;
  asm("v_pk_mul_f32 %0, %1, %2 op_sel:[0,0] op_sel_hi:[0,1]\n\tv_pk_fma_f32 %0, %1, %2, %0 op_sel:[1,1,0] op_sel_hi:[1,0,1] neg_lo:[0,1,0]"
      : "=&v"(r) : "v"(a), "v"(b));
  return r;
}
DEV f2 cexp2(float er, float ph) {
  const float f = __builtin_amdgcn_fractf(ph);
  return sp2(__builtin_amdgcn_exp2f(er)) * f2{__builtin_amdgcn_cosf(f), __builtin_amdgcn_sinf(f)};
}
template <int LB> DEV void dma2i(u32 la, u32 v0, u32 v1, v4u d, u32 soff) {
  asm volatile("s_add_u32 m0, %0, %1\n\ts_nop 0\n\tbuffer_load_dwordx4 %2, %4, %5 offen lds\n\t"
               "buffer_load_dwordx4 %3, %4, %5 offen offset:1024 lds" ::"s"(la), "i"(LB), "v"(v0), "v"(v1), "s"(d), "s"(soff) : "scc");
}
template <int OFF> DEV void addtid2(v2u v) {
  asm volatile("ds_write_addtid_b32 %0 offset:%2\n\tds_write_addtid_b32 %1 offset:%3" ::"v"(v.x), "v"(v.y), "i"(OFF), "i"(OFF + 256) : "memory");
}
template <int K> DEV void arrive_(u32 base) {
  asm volatile("s_mov_b64 exec, 1\n\tds_inc_u32 %0, %0 offset:%1\n\ts_mov_b64 exec, -1" ::"v"(base), "i"(4 * K) : "memory");
}
DEV void await(LAS u32* p, u32 n) {
NOUNROLL
  for (int spin = 0; spin < 64 * SPIN && (u32)__builtin_amdgcn_readfirstlane(*(volatile LAS u32*)p) < n; ++spin) {}
  CFENCE();
}
DEV u32 hplace(int j, int T, int c, int q, int u) {
  const int g = ((8 * j + 2 * q + u) * 147) >> 11, sh = (g & 1) ? -1 - (g >> 1) : 1 + (g >> 1);
  const int t2 = T * 16 + c + sh, tw = t2 & 2047;
  return (tw >> 4) * 14336 + q * 256 + (tw & 15) * 16 + u * 1024 + (t2 == tw ? 0u : 0x80000000u);
}
DEV int nxt7(int b8) { return b8 == 6 * 8192 ? 0 : b8 + 8192; }

__global__ void __launch_bounds__(512, 2)
fused_kernel(const u16*, const u16*, const u16*, const u16*, const u16*, const u16*, const u16*, const u16*, const u16*, const u16*,
             const u16*, const u16*, const u16*, u16*, unsigned char*) {
  __shared__ __attribute__((aligned(16))) char smem[163840];
  LAS char* const S = (LAS char*)smem;
  const u32 sb0 = (u32)(size_t)S;
  const int tid = threadIdx.x, lane = tid & 63, w = __builtin_amdgcn_readfirstlane(tid >> 6);
  u64 pc0;
  asm volatile("s_getpc_b64 %0" : "=s"(pc0));
  typedef u64 u64x8 __attribute__((ext_vector_type(8)));
  u64x8 ka;
  u64x8 kb;
  asm volatile("s_load_dwordx16 %0, %2, 0x38\n\ts_load_dwordx16 %1, %2, 0x0" : "=&s"(ka), "=&s"(kb) : "s"(__builtin_amdgcn_kernarg_segment_ptr()));
  const int c = lane & 15, q = lane >> 4, pa = (lane >> 2) & 3, pp = lane & 3;
  const int xcd = blockIdx.x & 7, rk = blockIdx.x >> 3;
  int cq = ((rk - 4) * 37) >> 8, j = (rk - 4) - 7 * cq, cl = xcd * 4 + cq;
  u32 wk = sb0 + w * 1024;
  const u32 rowo = (16 * (w & 3) + (lane >> 3)) * 896;
  u32 xo = rowo + 16 * ((lane & 7) ^ ((q + 6) & 7)) + cl * 57344, wo = rowo + 16 * ((lane & 7) ^ ((q & 1) | ((w & 1) << 1))) + j * 128, l16 = lane * 16;
  u32 xo1 = (xo ^ 64) + 6144, wo1 = wo + 6144, po = (64 * j + 2 * cl) * 512 + (tid & 255) * 4, wkb = sb0 + (w & 3) * 2048;
  int b80 = (j + 2 * cq - (j + 2 * cq >= 7 ? 7 : 0)) * 8192, b81 = nxt7(b80);
  int r0 = min(rk, 4) * 8 + w, r3 = w - (rk >> 2) * 8;
  int w7 = w, r4 = blockIdx.x;
  u32 v3o = (16 * (w & 3) + (lane >> 3)) * 3584 + (lane & 7) * 16;
  asm volatile("" : "+v"(v3o), "+v"(xo), "+v"(wo), "+v"(l16), "+v"(xo1), "+v"(wo1), "+v"(po));
  asm volatile("s_waitcnt lgkmcnt(0)" : "+s"(ka), "+s"(kb), "+s"(wk), "+s"(cq), "+s"(j), "+s"(cl), "+s"(wkb), "+s"(b80), "+s"(b81), "+s"(r0), "+s"(r3), "+s"(w7), "+s"(r4));
  const char *x = (const char*)kb[0], *w1 = (const char*)ka[2], *w2 = (const char*)ka[3], *w3 = (const char*)ka[4], *w4 = (const char*)ka[5];
  const u16 *ldt = (const u16*)ka[0], *dsk = (const u16*)ka[1];
  u16* const out = (u16*)ka[6];
  unsigned char* const ws = (unsigned char*)ka[7];
  GAS u32* const flags = (GAS u32*)ws;
  GAS u32* const pech = (GAS u32*)(ws + O_ECH) + blockIdx.x * 4;
  const __amdgpu_buffer_rsrc_t rs = __builtin_amdgcn_make_buffer_rsrc(ws, 0, WS_END, 0x00020000);
  const v4u ones_ = {0x3f803f80u, 0x3f803f80u, 0x3f803f80u, 0x3f803f80u};
#define LDWS(vo, so) __builtin_amdgcn_raw_buffer_load_b128(rs, vo, so, AUX_SC1)
#define STWS(v, vo, so) do { const v4u sv_ = (v); __builtin_amdgcn_raw_buffer_store_b128(sv_, rs, vo, so, AUX_SC1); asm volatile("s_nop 1" ::"v"(sv_) : "memory"); } while (0)
  CSTAMP(30);
  STAMP(0);

  const int sp = xcd * 4 + rk;
  const v4u dx = mkdesc(x), dw1 = mkdesc(w1);
  int b8 = nxt7(b81);
#define LDXW(b) do { dma2i<L_W2 + (b) * 8192>(wkb, xo, xo1, dx, b8 >> 6); dma2i<L_W1 + (b) * 8192>(wkb, wo, wo1, dw1, b8 * 7); b8 = nxt7(b8); } while (0)
  u32 ee = 0, en = 0, tc0 = 0, tc1 = 0, pr0 = 0, pr1 = 0, pr2 = 0, pr3 = 0, pr4 = 0, pr5 = 0;
  {
    if (r0 >= 36) {
      asm volatile("global_load_dword %0, %6, %7\n\tglobal_load_dword %1, %6, %8\n\tglobal_load_dword %2, %6, %9\n\t"
                   "global_load_dword %3, %6, %10\n\tglobal_load_dword %4, %6, %11\n\tglobal_load_dword %5, %6, %12"
                   : "=&v"(pr0), "=&v"(pr1), "=&v"(pr2), "=&v"(pr3), "=&v"(pr4), "=&v"(pr5)
                   : "v"(po), "s"(kb[1]), "s"(kb[2]), "s"(kb[3]), "s"(kb[4]), "s"(kb[5]), "s"(kb[6]));
      if (cl < 28) dma2i<L_C>(wkb, v3o, v3o + 8 * 3584 - 1024, mkdesc(w3), j * 229376 + cl * 128);
    }
    if ((r0 >> 2) == 8 && r3 < 0) {
      dma2i<L_W2>(wkb, xo, xo1, dx, b80 >> 6); dma2i<L_W1>(wkb, wo, wo1, dw1, b80 * 7);
      dma2i<L_W2 + 8192>(wkb, xo, xo1, dx, b81 >> 6); dma2i<L_W1 + 8192>(wkb, wo, wo1, dw1, b81 * 7);
    }
    if (w7 == 7) asm volatile("global_load_dword %0, %1, %2 sc1" : "=&v"(ee) : "v"(0), "s"(pech));
    if (r4 == 7) {
      STWS((v4u{0, 0, 0, 0}), tid * 16, 0);
      if (w7 < 4) {
        STWS((v4u{0, 0, 0, 0}), tid * 16, 8192);
        asm volatile("global_load_dword %0, %1, %2 sc1" : "=&v"(en) : "v"(tid * 16), "s"(ws + O_ECH));
      }
    }
    if (r3 == 7)
      asm volatile("global_load_dword %0, %2, %4\n\tglobal_load_dword %1, %3, %4" : "=&v"(tc0), "=&v"(tc1)
                   : "v"((rk * CODEL + lane) * 128), "v"((rk * CODEL + CODEL - 64 + lane) * 128), "s"(pc0 & ~255ull));
  }
#define W4PREF(wid) do { \
    wait_flags(flags + F_W4 + min(lane, 27), 0u); \
    NOUNROLL for (int n = (wid); n < 196; n += 28) { \
      const int k = (n * 147) >> 10, jj = n - 7 * k, bb = jj + (k >> 2); u32 t_; \
      asm volatile("global_load_dword %0, %1, %2" : "=&v"(t_) \
                   : "v"((lane >> 3) * 28672 + (lane & 7) * 128), "s"(ws + O_W4T + jj * 229376 + (bb >= 7 ? bb - 7 : bb) * 4096 + (k & 3) * 1024)); \
    } \
    VMWAIT(0); } while (0)

  if (rk < 4) {
    STAMP(4 + SPM);
    if (sp >= 28) {
      if (sp == 28) {
        if (w == 7) VMWAIT(2); else VMWAIT(0);
        asm volatile("" : "+v"(en));
        BAR();
        if (w == 4) {
NOUNROLL
          for (int k = 12; k < 32; ++k) STWS((v4u{0, 0, 0, 0}), l16, k * 1024);
          VMWAIT(0);
          if (lane == 0) stflag(flags + F_Z2, 1u);
        }
      }
      if (sp > 28 || w >= 4) {
        __builtin_amdgcn_s_sleep(100);
        W4PREF(rk == 0 ? w - 4 : rk * 8 - 4 + w);
        VMWAIT(0);
        asm volatile("" ::"v"(tc0), "v"(tc1), "v"(ee), "v"(en));
        return;
      }
      bool done = tid < 32 && (tid & 7) == 7;
NOUNROLL
      for (int spin = 0; spin < SPIN; ++spin) {
        if (!done) stflag((GAS u32*)(ws + O_ECH) + tid * 4, ++en);
        if (spin == 0) STAMP(1 + SPM);
        __builtin_amdgcn_s_sleep(40);
        done = done || ldflag(flags + F_ACK + tid) != 0u;
        if (__all(done)) break;
      }
      STAMP(2 + SPM);
      DUMP();
      return;
    }
    if (w == 7) {
      asm volatile("s_waitcnt vmcnt(0)" : "+v"(ee), "+v"(tc0), "+v"(tc1)::"memory");
NOUNROLL
      for (int spin = 0; spin < SPIN && __all(ldflag(pech) == ee); ++spin) __builtin_amdgcn_s_sleep(2);
      STAMP(1 + SPM);
      if (lane == 0) stflag(flags + F_ACK + blockIdx.x, 1u);
      BAR();
      DUMP();
      return;
    }
    const u32 lb = sb0 + w * 16384;
    __builtin_amdgcn_s_sleep(W4DLY);
    {
      const v4u d4 = mkdesc((const char*)w4 + sp * 64 * 896 + w * 128), d3 = mkdesc((const char*)w3 + w * 64 * 3584 + sp * 128);
      const u32 v4 = (lane >> 3) * 896 + (lane & 7) * 16, v3 = (lane >> 3) * 3584 + (lane & 7) * 16;
      unroll<8>([&](auto g) { dmai<8192 + g * 1024>(lb, v4, d4, g * 8 * 896); });
      LAS char* p = S + w * 16384 + 8192 + (8 * q + pa) * 128 + 32 * pp;
#pragma unroll
      for (int e = 0; e < 2; ++e) {
        VMWAIT(4);
        if (e == 0) STAMP(1 + SPM);
        v4u a[4];
#pragma unroll
        for (int k = 0; k < 4; ++k) a[k] = trfrag(p, e * 4096 + 8 * k, e * 4096 + 512 + 8 * k);
        const int fb = 2 * sp + e;
#pragma unroll
        for (int k = 0; k < 4; ++k) STWS(a[k], l16, O_W4T + (((w * 8 + (fb & 7)) * 7 + (fb >> 3)) * 4 + k) * 1024);
      }
      unroll<8>([&](auto g) { dmai<g * 1024>(lb, v3, d3, g * 8 * 3584); });
    }
    VMWAIT(8);
    STAMP(5 + SPM);
    BAR();
    if (tid == 0) stflag(flags + F_W4 + sp, 1u);
    STAMP(2 + SPM);
    VMWAIT(0);
    {
      LAS char* p = S + w * 16384 + (16 * q + pa) * 128 + 16 * pp;
#pragma unroll
      for (int et = 0; et < 4; ++et) {
        const int o = 64 * (et >> 1) + 8 * (et & 1);
        const f4 cs = mfma(trfrag(p, o + 1024, o + 1536), ones_, mfma(trfrag(p, o, o + 512), ones_, f4{0, 0, 0, 0}));
        if (c == 0) *(LAS f4*)(S + 114688 + w * 256 + (32 * (et >> 1) + 8 * q + 4 * (et & 1)) * 4) = cs;
      }
    }
    BAR();
    if (w == 0) {
      float s = 0.f;
#pragma unroll
      for (int k = 0; k < 7; ++k) s += *(LAS float*)(S + 114688 + k * 256 + lane * 4);
      __builtin_amdgcn_raw_buffer_store_b32(asu(s), rs, lane * 4, O_CS3 + sp * 256, AUX_SC1);
      VMWAIT(0);
      if (lane == 0) stflag(flags + F_W3 + sp, 1u);
    }
    STAMP(3 + SPM);
    W4PREF(rk * 7 + w);
    STAMP(6 + SPM);
    DUMP();
    return;
  }

  const int ti = w >> 1, u = w & 1;
  LAS u32* const cnt = (LAS u32*)(S + L_CNT);
  u32 cntb = sb0 + L_CNT;
  asm volatile("" : "+v"(cntb));
  const v4u sent4 = {SENT, SENT, SENT, SENT};
  const u32 w3b = O_W3T + (8 * j + w) * 28672, h2b = O_H2 + cl * 229376 + w * 1024;
  const v4u dws = mkdesc(ws);
  v4u W3[28];
#define LDW3(k) W3[k] = LDWS(l16 + ((k) & 3) * 1024, w3b + ((k) >> 2) * 4096)
#define CLRH2(k) STWS(sent4, l16, h2b + (k) * 57344 + j * 8192)
  GAS u32* const pf4 = lane < 32 ? flags + F_W4 + 4 * min(lane & 7, 6) + ti : flags + F_W3 + 4 * j + ti;

  if (w < 4) {
    const int T = cl * 4 + w;
#define BPRIO 2
    __builtin_amdgcn_s_setprio(BPRIO);
    if (tid < 20) cnt[tid] = 0;
    const int u0 = w >> 1;
    const u32 hoff[2] = {hplace(j, T, c, q, u0), hplace(j, T, c, q, u0 ^ 1)};
    const u32 hsw = O_HS + j * 2048, x1w = O_X1 + T * 14336 + j * 2048, stw = O_ST + (T * 7 + j) * 128;
    u32 wa[4];
    {
      const int fsw = (pa >> 1) | ((q & 1) << 1);
      const u32 base = (16 * q + pa) * 128 + (((2 * pp) ^ fsw) << 4) + ((pp & 1) << 3);
#pragma unroll
      for (int p = 0; p < 4; ++p) wa[p] = base ^ ((((p ^ w) >> 1) << 4) | (((p ^ w) & 1) << 3));
    }
    const bool sg = ((w ^ q) & 1) != 0;
    STAMP(1);
    v4u xn[2], W2[14];
    {
      v4u X[14], A[2][8], ones = ones_;
      asm volatile("" : "+v"(ones));
      const u32 xr = L_W2 + (16 * w + c) * 128 + (((2 * q) ^ (((c >> 1) + 6) & 7)) << 4);
      f4 G = {0, 0, 0, 0}, Sx = {0, 0, 0, 0}, acc[4] = {{0, 0, 0, 0}, {0, 0, 0, 0}, {0, 0, 0, 0}, {0, 0, 0, 0}}, cs = {0, 0, 0, 0};
      float mean, r1, rs2;
#define KOFF(ks) (((ks) >> 1) * 8192 + ((ks) & 1) * 1024)
      asm volatile("" : "+v"(wa[0]), "+v"(wa[1]), "+v"(wa[2]), "+v"(wa[3]));
      LDXW(2);
      const __amdgpu_buffer_rsrc_t rw2 = __builtin_amdgcn_make_buffer_rsrc((void*)w2, 0, 401408, 0x00020000);
#define LDW2(b) do { W2[2 * (b)] = __builtin_amdgcn_raw_buffer_load_b128(rw2, wo, b8 * 7, 0); \
        W2[2 * (b) + 1] = __builtin_amdgcn_raw_buffer_load_b128(rw2, wo1 + 1024, b8 * 7, 0); b8 = nxt7(b8); } while (0)
      unroll<8>([&](auto b_) {
        constexpr int b = b_;
        if constexpr (b <= 6) {
          if (b == 4) VMWAIT(10); else if (b == 5) VMWAIT(10); else if (b == 6) VMWAIT(10); else VMWAIT(8);
          if (b == 0) BAR(); else RBAR();
          X[2 * b] = *(LAS v4u*)(S + xr + b * 8192); X[2 * b + 1] = *(LAS v4u*)(S + (xr ^ 16) + b * 8192);
        }
        SB();
        if constexpr (b >= 1) { if (b <= 6) LGKM(2); else LGKM(0); }
        if constexpr (b == 7) { G = mfma(X[12], X[12], G); Sx = mfma(ones, X[12], Sx); G = mfma(X[13], X[13], G); Sx = mfma(ones, X[13], Sx); }
#pragma unroll
        for (int n = 0; n < 8; ++n) {
          if constexpr (b >= 1) acc[n & 3] = mfma(A[(b - 1) & 1][n], X[2 * b - 2 + (n >> 2)], acc[n & 3]);
          if constexpr (b <= 6) A[b & 1][n] = trfrag(S + L_W1 + wa[n & 3], KOFF(2 * b + (n >> 2)), KOFF(2 * b + (n >> 2)) + 512);
          if (b < 7) SB();
        }
        if constexpr (b >= 1) cs = mfma(A[(b - 1) & 1][0], ones, cs);
        if constexpr (b >= 1 && b <= 6) G = mfma(X[2 * b - 2], X[2 * b - 2], G);
        if (b < 7) SB();
        if constexpr (b <= 3) dma2i<L_W2 + (b + 3) * 8192>(wkb, xo, xo1, dx, b8 >> 6); else if constexpr (b <= 6) LDW2(2 * b - 8);
        if constexpr (b >= 1 && b <= 6) Sx = mfma(ones, X[2 * b - 2], Sx);
        if constexpr (b >= 1) cs = mfma(A[(b - 1) & 1][4], ones, cs);
        if (b < 7) SB();
        if constexpr (b <= 3) { dma2i<L_W1 + (b + 3) * 8192>(wkb, wo, wo1, dw1, b8 * 7); b8 = nxt7(b8); } else if constexpr (b <= 5) LDW2(2 * b - 7);
        if constexpr (b == 3) dma2i<L_XS>(wkb, xo, xo1, dx, j * 128);
        if constexpr (b >= 1 && b <= 6) { G = mfma(X[2 * b - 1], X[2 * b - 1], G); Sx = mfma(ones, X[2 * b - 1], Sx); }
        if (b == 7) {
          const float gd = __shfl((c & 2) ? ((c & 1) ? G[3] : G[2]) : ((c & 1) ? G[1] : G[0]), (c >> 2) * 16 + c);
          mean = Sx[0] * (1.f / 448.f);
          const float var = __builtin_fmaxf(__builtin_fmaf(gd, 1.f / 448.f, -mean * mean), 0.f);
          r1 = __builtin_amdgcn_rsqf(var + 1e-5f);
          rs2 = r1 * __builtin_amdgcn_rsqf(__builtin_fmaf(var * r1, r1, 1e-5f));
        }
        if (b == 0) STAMP(14);
        if (b == 2) STAMP(27);
        if (b == 4) STAMP(29);
        if (b == 6) STAMP(2);
        SB();
      });
      STAMP(4);
      if (c == 0) *(LAS f4*)(S + L_CS + w * 64 + q * 16) = cs;
      VMWAIT(10);
      arrive_<1>(cntb);
      LDW2(5); LDW2(6);
      await(cnt + 1, 4);
      STAMP(5);
      const f2 vm = sp2(-mean), vr = sp2(rs2);
      f4 cc[4];
#pragma unroll
      for (int p = 0; p < 4; ++p) cc[p] = *(LAS f4*)(S + L_CS + (p ^ w) * 64 + q * 16);
#pragma unroll
      for (int pi = 0; pi < 2; ++pi) {
        const f4 c0 = cc[2 * pi], c1 = cc[2 * pi + 1];
        const f4 o0 = acc[2 * pi], o1 = acc[2 * pi + 1];
        const u32 a0 = gelu_pk(fma2(c0.xy, vm, o0.xy) * vr), a1 = gelu_pk(fma2(c0.zw, vm, o0.zw) * vr);
        const u32 b0 = gelu_pk(fma2(c1.xy, vm, o1.xy) * vr), b1 = gelu_pk(fma2(c1.zw, vm, o1.zw) * vr);
        v4u hv = {sg ? b0 : a0, sg ? b1 : a1, sg ? a0 : b0, sg ? a1 : b1};
        if ((int)hoff[pi] < 0) hv = v4u{0, 0, 0, 0};
        STWS(hv, hoff[pi] & 0x7fffffffu, hsw);
      }
      STAMP(6);
#pragma unroll
      for (int pi = 0; pi < 2; ++pi) {
        const v4u xq = *(LAS v4u*)(S + ((xr - L_W2 + L_XS) ^ ((pi ^ u0) * 16)));
#pragma unroll
        for (int k = 0; k < 4; ++k) xn[pi][k] = pk2(fma2(unpk(xq[k]), sp2(r1), sp2(-mean * r1)));
      }
      LAS char* xt = S + L_XT + (16 * q) * 144 + (16 * w + c) * 2;
#pragma unroll
      for (int k = 0; k < 8; ++k) {
        *(LAS u16*)(xt + ((((k >> 2) ^ u0) * 8) + 2 * (k & 3)) * 144) = (u16)xn[k >> 2][k & 3];
        *(LAS u16*)(xt + ((((k >> 2) ^ u0) * 8) + 2 * (k & 3) + 1) * 144) = (u16)(xn[k >> 2][k & 3] >> 16);
      }
      arrive_<0>(cntb);
    }
    {
      GAS u32* const pf1 = lane < 24 ? flags + F_CLR + (((cl + (lane >> 3) - 1) & 31) * 8 + min(lane & 7, 6)) * 4 : lane < 48 ? flags + F_CLR + ((4 * j + ti) * 8 + min(lane & 7, 6)) * 4 : flags + F_Z2;
      const u32 fl1 = ldflag(pf1);
      await(cnt + 0, 4);
      {
        LAS char* xr2 = S + L_XT + (16 * w + (lane >> 3)) * 144 + (lane & 7) * 16;
        const v4u t0 = *(LAS v4u*)(xr2), t1 = *(LAS v4u*)(xr2 + 8 * 144);
        const u32 xnto = (16 * w + (lane >> 3)) * 4096 + (lane & 7) * 16, xntb = O_XNT + j * 262144 + cl * 128;
        STWS(t0, xnto, xntb); STWS(t1, xnto, xntb + 32768);
      }
      STAMP(3);
      wait_flags(pf1, fl1);
      STAMP(7);
      v4u H[14];
      const u32 hsb = O_HS + T * 14336;
#define LDHS() do { int h8 = b80; _Pragma("unroll") for (int b = 0; b < 7; ++b) { \
        H[2 * b] = LDWS(l16, hsb + (h8 >> 2)); H[2 * b + 1] = LDWS(l16 + 1024, hsb + (h8 >> 2)); h8 = nxt7(h8); } } while (0)
#define HSBAD() __any(max3u(max3u(max3u(H[0].x, H[1].x, H[2].x), max3u(H[3].x, H[4].x, H[5].x), max3u(H[6].x, H[7].x, H[8].x)), \
                            max3u(H[9].x, H[10].x, H[11].x), max(H[12].x, H[13].x)) == SENT)
      LDHS();
      SB();
#pragma unroll
      for (int k = 0; k < 14; ++k) *(LAS v4u*)(S + L_W2 + (k >> 1) * 8192 + w * 2048 + (k & 1) * 1024 + l16) = W2[k];
      arrive_<2>(cntb);
#pragma unroll
      for (int k = 0; k < NCB; ++k) CLRH2(k);
      STWS(sent4, l16, x1w); STWS(sent4, l16 + 1024, x1w);
      if (q == 0) __builtin_amdgcn_raw_buffer_store_b64(v2u{SENT, SENT}, rs, c * 8, stw, AUX_SC1);
      await(cnt + 2, 4);
      STAMP(8);
      asm volatile("s_waitcnt vmcnt(%0)" ::"i"(17 + NCB) : "memory");
      if (lane == 0) stflag(flags + F_XT + (j * 4 + w) * 32 + cl, 1u);
      v4u A[3][4];
#pragma unroll
      for (int p = 0; p < 8; ++p) A[p >> 2][p & 3] = trfrag(S + L_W2 + wa[p & 3], KOFF(p >> 2), KOFF(p >> 2) + 512);
      SB();
      f4 acc[4] = {{0, 0, 0, 0}, {0, 0, 0, 0}, {0, 0, 0, 0}, {0, 0, 0, 0}};
#pragma unroll
      for (int ks = 0; ks < 14; ++ks) {
        if (ks < 13) LGKM(8); else LGKM(0);
#pragma unroll
        for (int p = 0; p < 4; ++p) {
          acc[p] = mfma(A[ks % 3][p], H[ks], acc[p]);
          if (ks < 12) A[(ks + 2) % 3][p] = trfrag(S + L_W2 + wa[p], KOFF(ks + 2), KOFF(ks + 2) + 512);
          if (p == 3 && ks >= 14 - NB3) LDW3(ks - 14 + NB3);
          SB();
        }
      }
      if (HSBAD()) {
NOUNROLL
        for (int spin = 0; spin < SPIN; ++spin) { RETRY(3); CFENCE(); LDHS(); if (!HSBAD()) break; }
#pragma unroll
        for (int p = 0; p < 4; ++p) acc[p] = f4{0, 0, 0, 0};
#pragma unroll
        for (int ks = 0; ks < 14; ++ks) {
#pragma unroll
          for (int p = 0; p < 4; ++p) acc[p] = mfma(trfrag(S + L_W2 + wa[p], (ks >> 1) * 8192 + (ks & 1) * 1024, (ks >> 1) * 8192 + (ks & 1) * 1024 + 512), H[ks], acc[p]);
          SB();
        }
      }
      arrive_<3>(cntb);
      asm volatile("s_waitcnt vmcnt(%0)" ::"i"(NB3) : "memory");
      if (lane == 0) stflag(flags + F_C1 + (cl * 4 + w) * 8 + j, 1u);
      STAMP(9);
      float s1 = 0.f, s2 = 0.f;
      v4u x1p[2];
#pragma unroll
      for (int pi = 0; pi < 2; ++pi) {
        const f4 o0 = acc[2 * pi], o1 = acc[2 * pi + 1];
        const v4u n = xn[pi];
        const u32 n0 = sg ? n[2] : n[0], n1 = sg ? n[3] : n[1], n2 = sg ? n[0] : n[2], n3 = sg ? n[1] : n[3];
        const u32 a0 = pks(o0.x + blo(n0), o0.y + bhi(n0)), a1 = pks(o0.z + blo(n1), o0.w + bhi(n1));
        const u32 b0 = pks(o1.x + blo(n2), o1.y + bhi(n2)), b1 = pks(o1.z + blo(n3), o1.w + bhi(n3));
        x1p[pi] = v4u{sg ? b0 : a0, sg ? b1 : a1, sg ? a0 : b0, sg ? a1 : b1};
        STWS(x1p[pi], l16, x1w + (pi ^ u0) * 1024);
      }
#pragma unroll
      for (int k = 0; k < 8; ++k) {
        const float e0 = blo(x1p[k >> 2][k & 3]), e1 = bhi(x1p[k >> 2][k & 3]);
        s1 = adds(s1, adds(e0, e1));
        s2 = __builtin_fmaf(e0, e0, __builtin_fmaf(e1, e1, s2));
      }
      s1 += __shfl_xor(s1, 16); s2 += __shfl_xor(s2, 16);
      s1 += __shfl_xor(s1, 32); s2 += __shfl_xor(s2, 32);
      if (q == 0) __builtin_amdgcn_raw_buffer_store_b64(v2u{asu(s1), asu(s2)}, rs, c * 8, stw, AUX_SC1);
      STAMP(10);
      SB();
#pragma unroll
      for (int k = NB3; k < NB3 + NB3B; ++k) LDW3(k);
      SB();
      await(cnt + 6, 4);
#pragma unroll
      for (int pi = 0; pi < 2; ++pi) *(LAS v4u*)(S + L_X1S + w * 2048 + (pi ^ u0) * 1024 + l16) = x1p[pi];
      await(cnt + 3, 4);
      v2u sa, sb;
      const u32 x1b = O_X1 + T * 14336, x1l = L_W2 + w * 14336;
#define LDX1() do { \
      unroll<14>([&](auto b) { dmai_sc1<b * 1024>(sb0 + x1l, l16, dws, x1b + b * 1024); }); \
      sa = __builtin_amdgcn_raw_buffer_load_b64(rs, q * 128 + c * 8, O_ST + T * 896, AUX_SC1); \
      sb = __builtin_amdgcn_raw_buffer_load_b64(rs, min(q + 4, 6) * 128 + c * 8, O_ST + T * 896, AUX_SC1); } while (0)
      GAS u32* const pf3 = flags + F_C1 + (cl * 4 + w) * 8 + min(lane & 7, 6);
      wait_flags(pf3, ldflag(pf3));
      STAMP(11);
      LDX1();
      SB();
#pragma unroll
      for (int k = NB3 + NB3B; k < NB3 + NB3B + NB3C; ++k) LDW3(k);
      SB();
      asm volatile("s_waitcnt vmcnt(%2)" : "+v"(sa), "+v"(sb) : "i"(NB3C) : "memory");
NOUNROLL
      for (int spin = 0; spin < SPIN; ++spin) {
        u32 m = max(sa.x, sb.x);
#pragma unroll
        for (int b = 0; b < 14; ++b) m = max(m, *(LAS u32*)(S + x1l + b * 1024 + l16));
        if (!__any(m == SENT)) break;
        RETRY(5);
        LDX1();
        asm volatile("s_waitcnt vmcnt(0)" : "+v"(sa), "+v"(sb)::"memory");
      }
      s1 = asf(sa.x) + (q < 3 ? asf(sb.x) : 0.f); s2 = asf(sa.y) + (q < 3 ? asf(sb.y) : 0.f);
      s1 += __shfl_xor(s1, 16); s2 += __shfl_xor(s2, 16);
      s1 += __shfl_xor(s1, 32); s2 += __shfl_xor(s2, 32);
      const float mean = s1 * (1.f / 448.f);
      const float v1 = __builtin_fmaxf(__builtin_fmaf(s2, 1.f / 448.f, -mean * mean), 0.f) + 1e-5f, rstd = __builtin_amdgcn_rsqf(v1);
      if (q == 0) *(LAS f4*)(S + L_ST + (w * 16 + c) * 16) = f4{-mean, -2.302208198f * rstd, -0.1029432397f * rstd * rstd * rstd, v1 * rstd};
      STAMP(12);
      SB();
#pragma unroll
      for (int k = NCB; k < 4; ++k) CLRH2(k);
#pragma unroll
      for (int k = NB3 + NB3B + NB3C; k < 28; ++k) LDW3(k);
      SB();
    }
  } else {
    const int ch = (w >> 1) & 1, dch = 64 * j + 2 * cl + ch, tp = tid & 127;
    const u32 ldw = ((const CAS u32*)ldt)[dch >> 1], dkw = ((const CAS u32*)dsk)[dch >> 1];
    const float dt = blo(pk(expf((dch & 1) ? bhi(ldw) : blo(ldw)), 0.f));
    const float dskip = (dch & 1) ? bhi(dkw) : blo(dkw);
    {
      const u32 h0 = hplace(j, cl * 4 + w - 4, c, q, 0) & 0x7fffffffu, h1 = hplace(j, cl * 4 + w - 4, c, q, 1) & 0x7fffffffu;
      __builtin_amdgcn_s_setprio(3);
      STWS(sent4, h0, O_HS + j * 2048); STWS(sent4, h1, O_HS + j * 2048);
      __builtin_amdgcn_s_setprio(0);
    }
    { float dt_ = dt; asm volatile("" : "+v"(dt_)); }
#define JOIN() do { SB(); RBAR(); SB(); } while (0)
    asm volatile("s_waitcnt vmcnt(2)" : "+v"(pr0), "+v"(pr1), "+v"(pr2), "+v"(pr3), "+v"(pr4), "+v"(pr5), "+v"(ee)::"memory");
    JOIN();
    if (cl < 28) {
      LAS char* p = S + L_C + (16 * q + pa) * 128 + 16 * pp + 64 * ch + 8 * u;
      const v4u a0 = trfrag(p, 0, 512), a1 = trfrag(p, 1024, 1536);
      __builtin_amdgcn_s_setprio(3);
      STWS(a0, l16, O_W3T + (((2 * cl + ch) * 14 + 2 * j) * 2 + u) * 1024);
      STWS(a1, l16 + 2048, O_W3T + (((2 * cl + ch) * 14 + 2 * j) * 2 + u) * 1024);
      __builtin_amdgcn_s_setprio(0);
    }
    STAMP(1 + APM);
    LAS char* const fb = S + (w - 4) * FBS;
    GAS u32* const pf5 = flags + F_CLR + ((4 * j + ti) * 8 + min(lane & 7, 6)) * 4;
    u32 fl5;
    {
      LAS char* const fr = fb + c * 544 + q * 16;
      struct c2 { f2 a, b; };
      auto mul = [](c2 x_, c2 y_) { return c2{cmul(x_.a, y_.a), cmul(x_.b, y_.b)}; };
      v2u fv[57];
#define PUT(i, z) do { const c2 z_ = (z); fv[i] = v2u{pk2(z_.a), pk2(z_.b)}; } while (0)
      const f2 er = unpk(pr0) * sp2(dt * 1.4426950409f), ph = (unpk(pr1) * sp2(dt)) * sp2(0.15915494309f);
      const f2 br = unpk(pr2) * sp2(dt), bi = unpk(pr3) * sp2(-dt), cr = unpk(pr4), ci = -unpk(pr5);
      const c2 cb = mul(c2{f2{cr.x, ci.x}, f2{cr.y, ci.y}}, c2{f2{br.x, bi.x}, f2{br.y, bi.y}});
#define PIN(i, z) asm volatile("" : "+v"(fv[i]), "+v"((z).b))
      const c2 p1 = {cexp2(er.x, ph.x), cexp2(er.y, ph.y)};
      c2 p2 = mul(p1, p1);
      asm volatile("" : "+v"(p2.b));
      JOIN();
      const c2 p4 = mul(p2, p2), p8 = mul(p4, p4), p16 = mul(p8, p8);
      const c2 g1 = {cexp2(er.x * 256.f, ph.x * 256.f), cexp2(er.y * 256.f, ph.y * 256.f)};
      STAMP(10 + APM);
      {
        const c2 wc = {f2{p16.a.x, -p16.a.y}, f2{p16.b.x, -p16.b.y}};
        c2 r = cb;
#pragma unroll
        for (int i = 0; i < 16; ++i) { PUT(i, r); if (i < 15) r = mul(r, wc); if (i == 4) { PIN(i, r); JOIN(); } }
        asm volatile("" : "+v"(fv[15]));
        JOIN();
        c2 z = p1, y = p2;
#pragma unroll
        for (int i = 0; i < 4; ++i) {
          PUT(16 + 2 * i, z); PUT(17 + 2 * i, mul(z, p2)); PUT(24 + i, y); PUT(28 + i, mul(y, g1));
          if (i < 3) { z = mul(z, p4); y = mul(y, p4); }
          if (i == 2) {
            PIN(30, y);
            SB();
            if (w == 7) asm volatile("global_load_dword %0, %1, %2 sc1" : "=&v"(en) : "v"(0), "s"(pech));
            JOIN();
          }
        }
        const c2 g2 = mul(g1, g1), g3 = mul(g2, g1), p12 = mul(p4, p8);
        PUT(32, p4); PUT(33, p12); PUT(34, mul(p4, g1)); PUT(35, mul(p12, g1)); PUT(36, mul(p4, g2)); PUT(37, mul(p12, g2));
        asm volatile("" : "+v"(fv[31]), "+v"(fv[23]), "+v"(fv[35]), "+v"(fv[34]), "+v"(fv[36]), "+v"(fv[37]));
        SB();
        VMWAIT(0);
        JOIN();
        if (w == 7) {
          asm volatile("" : "+v"(en), "+v"(ee));
          if (__all(en == ee)) {
NOUNROLL
            for (int spin = 0; spin < SPIN && __all(ldflag(pech) == ee); ++spin) RETRY(0);
          }
          if (lane == 0) { stflag(flags + F_CLR + (cl * 8 + j) * 4, 1u); stflag(flags + F_ACK + blockIdx.x, 1u); }
        }
        PUT(38, mul(p4, g3)); PUT(39, mul(p12, g3));
        fv[40] = v2u{0x3f80u, 0x3f80u};
        PUT(41, p8); PUT(42, g1); PUT(43, mul(p8, g1)); PUT(44, g2); PUT(45, mul(p8, g2)); PUT(46, g3); PUT(47, mul(p8, g3));
        asm volatile("" : "+v"(fv[39]), "+v"(fv[43]), "+v"(fv[45]), "+v"(fv[47]), "+v"(fv[38]));
        STAMP(11 + APM);
        JOIN();
        z = mul(g2, g2);
#pragma unroll
        for (int i = 0; i < 4; ++i) { PUT(48 + 2 * i, z); PUT(49 + 2 * i, mul(z, p8)); z = mul(z, g1); }
        PUT(56, z);
      }
      STAMP(13 + APM);
      v4u KA[8], B[8];
      f4 kp0 = {0, 0, 0, 0}, kp1 = {0, 0, 0, 0}, kp2 = {0, 0, 0, 0};
      const u32 fbm = sb0 + (w - 4) * FBS;
      asm volatile("s_mov_b32 m0, %0\n\ts_nop 0" ::"s"(fbm));
#define FPUT(i0, n) unroll<n>([&](auto i) { addtid2<i * 544>(fv[(i0) + i]); })
      FPUT(0, 16);
#pragma unroll
      for (int s = 0; s < 8; ++s) KA[s] = *(LAS v4u*)(fr + s * 64);
      FS(19);
      FPUT(16, 16);
#pragma unroll
      for (int s = 0; s < 8; ++s) B[s] = *(LAS v4u*)(fr + s * 64);
      FS(20);
      FPUT(32, 16);
#pragma unroll
      for (int s = 0; s < 8; ++s) kp0 = mfma(KA[s], B[s], kp0);
#pragma unroll
      for (int s = 0; s < 8; ++s) B[s] = *(LAS v4u*)(fr + s * 64);
      FS(21);
      fl5 = ldflag(pf5);
      FPUT(48, 9);
#pragma unroll
      for (int s = 0; s < 8; ++s) kp1 = mfma(KA[s], B[s], kp1);
#pragma unroll
      for (int s = 0; s < 8; ++s) kp2 = mfma(KA[s], *(LAS v4u*)(fr + s * 64), kp2);
      CFENCE();
      LAS char* pw = fb + c * 64 + q * 16;
      *(LAS f4*)(pw) = kp0; *(LAS f4*)(pw + 1024) = kp1; *(LAS f4*)(pw + 2048) = kp2;
#undef PUT
    }
    arrive_<4>(cntb);
    wait_flags(pf5, fl5);
#pragma unroll
    for (int k = 0; k < NA3; ++k) LDW3(k);
    SB();
    FS(22);
    STAMP(4 + APM);
    __builtin_amdgcn_s_setprio(0);
    await(cnt + 1, 4);
    await(cnt + 4, 4);
    {
      const int t4 = tid - 256;
      if (t4 < 32) *(LAS v4u*)(S + L_KC + (t4 >> 2) * KCS + 4096 + (t4 & 3) * 16) = v4u{0, 0, 0, 0};
      else if (t4 < 34) *(LAS v2u*)(S + L_KT + (t4 - 32) * KTS) = v2u{0, 0};
    }
#pragma unroll
    for (int rr = 0; rr < 2; ++rr) {
      const int e = tp + 128 * rr;
      if (e < 164) {
        LAS char* pp4 = S + L_C + ch * 2 * FBS + e * 16;
        const f4 v = *(LAS f4*)(pp4) + *(LAS f4*)(pp4 + FBS);
        const int col = e >> 2, k8 = col & 7;
        const int cv = col < 8 ? 2 * col + 1 : col < 16 ? 2 + 4 * (col & 3) + 64 * (k8 & 4) : 8 * (col & 1) + (col < 24 ? 4 + 128 * (k8 & 6) : 128 * ((col - 24) & 30));
        LAS char* kt = S + L_KT + ch * KTS + 8 + cv * 2 + (e & 3) * 128;
        const u32 k01 = pk(v[0], v[1]), k23 = pk(v[2], v[3]);
        if (e <= 160) *(LAS u16*)(kt) = (u16)k01;
        if (e < 160) { *(LAS u16*)(kt + 32) = (u16)(k01 >> 16); *(LAS u16*)(kt + 64) = (u16)k23; *(LAS u16*)(kt + 96) = (u16)(k23 >> 16); }
      }
    }
    arrive_<6>(cntb);
#pragma unroll
    for (int k = NA3; k < NA3 + NA3B; ++k) LDW3(k);
    SB();
    STAMP(5 + APM);
    await(cnt + 6, 4);
    GAS u32* const pf2 = lane < 32 ? flags + F_XT + (j * 4 + (cl >> 3)) * 32 + lane : lane < 48 ? flags + F_CLR + ((4 * j + ti) * 8 + min(lane & 7, 6)) * 4 : flags + F_Z2;
    u32 fl2 = 0;
    LAS char* const ktb = S + L_KT + 8 + ch * KTS;
    LAS char* const kcb = S + L_KC + ch * 4 * KCS + tp * 16;
    {
      u32 v[11], pr[10];
      const float m0 = (float)(1023 - 8 * tp);
#pragma unroll
      for (int k = 0; k < 12; k += 2) {
        const u32 r = pk(m0 - (float)k, m0 - (float)(k + 1));
        v[k] = *(LAS u16*)(ktb + 2 * (int)blo(r));
        if (k < 10) v[k + 1] = *(LAS u16*)(ktb + 2 * (int)bhi(r));
      }
      const u32 hi = *(LAS u16*)(ktb + 4096 - 16 * tp), lo = *(LAS u16*)(ktb + 4080 - 16 * tp), l2 = *(LAS u16*)(ktb + (tp == 127 ? 2040 : 4080 - 16 * tp));
      SB();
      fl2 = ldflag(pf2);
      SB();
      const u32 t = (tp & 1) ? lo : hi, hh = hi * 0x10001u, ll = lo * 0x10001u, ht = hi | (t << 16), tl = t | (lo << 16);
      *(LAS v4u*)(kcb) = v4u{tl, ll, ll, lo | (l2 << 16)};
      *(LAS v4u*)(kcb + KCS) = v4u{ht, ll, ll, ll};
      *(LAS v4u*)(kcb + 2 * KCS) = v4u{hh, tl, ll, ll};
      *(LAS v4u*)(kcb + 3 * KCS) = v4u{hh, ht, ll, ll};
#pragma unroll
      for (int k = 0; k < 10; ++k) pr[k] = v[k] | (v[k + 1] << 16);
#pragma unroll
      for (int k = 0; k < 4; ++k) *(LAS v4u*)(kcb + 2048 + k * KCS) = v4u{pr[3 - k], pr[5 - k], pr[7 - k], pr[9 - k]};
    }
    STAMP(7 + APM);
    __builtin_amdgcn_s_setprio(3);
    wait_flags(pf2, fl2);
    v4u uv0 = LDWS(tp * 16, O_XNT + dch * 4096), uv1 = LDWS(tp * 16 + 2048, O_XNT + dch * 4096);
    SB();
    __builtin_amdgcn_s_setprio(0);
    STAMP(6 + APM);
    await(cnt + 2, 4);
    if (tp < 64) *(LAS v4u*)(S + L_U + ch * 5120 + (tp >> 4) * 1280 + (tp & 15) * 16) = v4u{0, 0, 0, 0};
    *(LAS v4u*)(S + L_U + ch * 5120 + (tp & 3) * 1280 + 256 + (tp >> 2) * 16) = uv0;
    *(LAS v4u*)(S + L_U + ch * 5120 + (tp & 3) * 1280 + 768 + (tp >> 2) * 16) = uv1;
    arrive_<7>(cntb);
    await(cnt + 7, 4);
    STAMP(8 + APM);
    LAS char* pa_ = S + L_KC + (ch * 4 + (c & 3)) * KCS + 2 * (2044 - (c & 12) + 8 * q - 16 * u) - 3072 - 960;
    LAS char* pb_ = S + L_U + ch * 5120 + q * 1280 + (1 + c) * 16;
    f4 y[4] = {{0, 0, 0, 0}, {0, 0, 0, 0}, {0, 0, 0, 0}, {0, 0, 0, 0}};
    v4u B[2][4], A[2][4];
#define CLDB(it, a) B[(it) & 1][a] = *(LAS v4u*)(pb_ + (15 - (it)) * 16 + (a) * 256)
#define CLDA(it, a) do { const v2u lo = *(volatile LAS v2u*)(pa_ + (15 - (it)) * 64 + (3 - (a)) * 1024), hi = *(volatile LAS v2u*)(pa_ + (15 - (it)) * 64 + (3 - (a)) * 1024 + 8); \
      A[(it) & 1][a] = v4u{lo.x, lo.y, hi.x, hi.y}; } while (0)
#pragma unroll
    for (int a = 0; a < 4; ++a) { CLDB(0, a); CLDA(0, a); }
    SB();
#pragma unroll
    for (int it = 0; it < 16; ++it) {
      int n = 0;
      LGKM(0);
#pragma unroll
      for (int a = 0; a < 4; ++a)
#pragma unroll
        for (int k = a; k < 4; ++k) {
          y[k] = mfma(A[it & 1][a], B[it & 1][k - a], y[k]);
          if (it < 15 && n < 4) CLDA(it + 1, n);
          if (it < 15 && (n == 4 || n == 5)) { CLDB(it + 1, 2 * n - 8); CLDB(it + 1, 2 * n - 7); }
          if (n == 8 && NA3 + NA3B + it < 28) LDW3(NA3 + NA3B + it);
          if (n == 7 && it < 8 && (it & 1)) CLRH2(it >> 1);
          SB();
          ++n;
        }
    }
    STAMP(9 + APM);
#pragma unroll
    for (int k = 0; k < 4; ++k) {
      const v2u uu = *(volatile LAS v2u*)(S + L_U + ch * 5120 + (2 * u + (q >> 1)) * 1280 + (16 + c) * 16 + (q & 1) * 8 + k * 256);
      *(LAS v2u*)(S + L_SR + ch * 4096 + c * 64 + u * 32 + q * 8 + k * 1024) =
          v2u{pk2(fma2(unpk(uu.x), sp2(dskip), y[k].xy)), pk2(fma2(unpk(uu.y), sp2(dskip), y[k].zw))};
    }
    __builtin_amdgcn_s_waitcnt(0x0F70);
    STAMP(12 + APM);
  }
  {
    const u32 fl4 = ldflag(pf4);
    SB();
    BAR();
    STAMP(15);
    STWS(*(LAS v4u*)(S + L_SR + tid * 16), (tid & 255) * 16, O_ST2 + (64 * j + 2 * cl + (w >> 2)) * 4096);
    wait_flags(pf4, fl4);
    STAMP(16);
  }
  const f4 cs3a = __builtin_bit_cast(f4, LDWS(q * 32, O_CS3 + (8 * j + w) * 128));
  const f4 cs3b = __builtin_bit_cast(f4, LDWS(q * 32 + 16, O_CS3 + (8 * j + w) * 128));

  v4u W4[28], own[4], H2[4][6];
  int bo[7];
  bo[0] = j * 8192;
#pragma unroll
  for (int b = 1; b < 7; ++b) bo[b] = nxt7(bo[b - 1]);
  const u32 w4b = O_W4T + (j * 8 + w) * 28672;
#define LDW4(k) W4[k] = LDWS(l16 + ((k) & 3) * 1024, w4b + (bo[(k) >> 2] >> 1))
#define DMW4(k) dma_sc1(sb0 + L_W1 + w * 7168, ((k) - 14) * 1024, l16, dws, w4b + (bo[(k) >> 2] >> 1) + ((k) & 3) * 1024)
#define LDH2(s) _Pragma("unroll") for (int b = 1; b < 7; ++b) H2[s][b - 1] = LDWS(l16, h2b + ((ti + 1 + (s)) & 3) * 57344 + bo[b])
#define H2BAD(s) __any(max3u(max3u(H2[s][0].x, H2[s][1].x, H2[s][2].x), max3u(H2[s][3].x, H2[s][4].x, H2[s][5].x), 0u) == SENT)
#define CKH2(s) if (H2BAD(s)) { NOUNROLL for (int spin = 0; spin < SPIN; ++spin) { RETRY(6); CFENCE(); LDH2(s); if (!H2BAD(s)) break; } }
  GAS u32* const pfc = flags + F_C2 + (cl * 8 + w) * 8 + min(lane & 7, 6);
  u32 flc = 0;
  if (w < 4) __builtin_amdgcn_s_setprio(P3B);
  v4u Bq[4];
#pragma unroll
  for (int k = 0; k < 3; ++k) Bq[k] = *(LAS v4u*)(S + L_W2 + ((ti + 1) & 3) * 14336 + l16 + k * 1024);
#pragma unroll
  for (int s = 0; s < 4; ++s) {
    const int tl = (ti + 1 + s) & 3;
    LAS char* ap = S + L_W2 + tl * 14336 + l16;
    LAS char* an = S + L_W2 + ((tl + 1) & 3) * 14336 + l16;
    const f4 st = *(LAS f4*)(S + L_ST + (tl * 16 + c) * 16);
    f4 a0 = {0, 0, 0, 0}, a1 = {0, 0, 0, 0};
#pragma unroll
    for (int ks = 0; ks < 14; ++ks) {
      const int qi = (s * 14 + ks) & 3, qn = (s * 14 + ks + 3) & 3;
      a0 = mfma(W3[2 * ks], Bq[qi], a0);
      if (ks + 3 < 14) Bq[qn] = *(LAS v4u*)(ap + (ks + 3) * 1024);
      else if (s < 3) Bq[qn] = *(LAS v4u*)(an + (ks - 11) * 1024);
      SB();
      a1 = mfma(W3[2 * ks + 1], Bq[qi], a1);
      if (s == 0 && ks >= 6 && !(ks & 1)) LDW4(ks / 2 - 3);
      if (s == 1 && !(ks & 1)) LDW4(4 + ks / 2);
      if (s == 2 && !(ks & 1)) { if (ks < 6) LDW4(11 + ks / 2); else DMW4(11 + ks / 2); }
      if (s == 3) { if (ks < 3) DMW4(18 + ks); else if (ks >= 5 && ks < 12) LDW4(16 + ks); }
      SB();
    }
    if (s == 0) {
      VMWAIT(6);
      if (lane == 0) { stflag(flags + F_C2 + (cl * 8 + w) * 8 + j, 1u); stflag(flags + F_S + j * 256 + (w & 3) * 64 + cl * 2 + (w >> 2), 1u); }
    }
    if (s == 2) { flc = ldflag(pfc); SB(); }
    own[s] = v4u{pks(gelu_s(a0.x, cs3a.x, st), gelu_s(a0.y, cs3a.y, st)), pks(gelu_s(a0.z, cs3a.z, st), gelu_s(a0.w, cs3a.w, st)),
                 pks(gelu_s(a1.x, cs3b.x, st), gelu_s(a1.y, cs3b.y, st)), pks(gelu_s(a1.z, cs3b.z, st), gelu_s(a1.w, cs3b.w, st))};
    __builtin_amdgcn_s_setprio(3);
    STWS(own[s], l16, h2b + tl * 57344 + bo[0]);
    if (w < 4) __builtin_amdgcn_s_setprio(P3B); else __builtin_amdgcn_s_setprio(0);
    SB();
    if (s == 2) {
      wait_flags(pfc, flc);
    }
    if (s >= 2) { LDH2(s - 2); SB(); }
    LS(17 + s);
  }
  VMWAIT(14);
#pragma unroll
  for (int k = 14; k < 21; ++k) W4[k] = *(LAS v4u*)(S + L_W1 + w * 7168 + (k - 14) * 1024 + l16);
  GAS u32* const pfs = flags + F_S + j * 256 + (cl >> 3) * 64 + lane;
  const u32 fls = ldflag(pfs);
  f4 o[4][4];
#pragma unroll
  for (int s = 0; s < 4; ++s)
#pragma unroll
    for (int t = 0; t < 4; ++t) o[s][t] = mfma(W4[t], own[s], f4{0, 0, 0, 0});
  SB();
  LS(21);
  float pre[8];
  v4u xq, sp_;
  u32 lz = l16;
  LAS char* const rown = S + L_W1 + ti * 28672 + u * 2048 + l16;
#pragma unroll
  for (int s = 0; s < 4; ++s) {
    CKH2(s);
    LAS char* const rw = S + L_W1 + (((ti + s) & 3) * 7 + 2 * s - 2 + u) * 4096 + l16;
#pragma unroll
    for (int b = 1; b < 7; ++b) {
      f4 r0, r1;
      if (s == 3 && b == 1) {
        asm volatile("" : "+v"(lz));
        const v4u x1p = *(LAS v4u*)(S + L_X1S + w * 1024 + l16);
#pragma unroll
        for (int k = 0; k < 4; ++k) { pre[2 * k] = blo(x1p[k]); pre[2 * k + 1] = bhi(x1p[k]); }
      }
      if (s == 3 && b >= 2 && b <= 5) {
        r0 = *(LAS f4*)(rown + (b - 2) * 4096); r1 = *(LAS f4*)(rown + (b - 2) * 4096 + 1024);
      }
      if (s == 3 && b == 6) {
        xq = *(LAS v4u*)(S + L_XS + ti * 2048 + ((lz >> 4) & 15) * 128 + ((((lz >> 7) & 6) + u) ^ ((((lz >> 5) & 7) + 6) & 7)) * 16);
        sp_ = trfrag(S + L_SB + u * 1024 + ti * 32 + (lz >> 8) * 2048 + ((lz >> 6) & 3) * 128 + ((lz >> 4) & 3) * 8, 0, 512);
      }
#pragma unroll
      for (int t = 0; t < 4; ++t) o[s][t] = mfma(W4[4 * b + t], H2[s][b - 1], o[s][t]);
      if (s >= 1 && b <= 4) *(LAS f4*)(rw + (b - 1) * 1024) = o[s - 1][b - 1];
      if (s == 3 && b >= 2 && b <= 5) {
#pragma unroll
        for (int k = 0; k < 4; ++k) { pre[k] = adds(pre[k], r0[k]); pre[4 + k] = adds(pre[4 + k], r1[k]); }
      }
      SB();
    }
    if (s < 2) { LDH2(s + 2); SB(); }
    if (s == 0) {
      wait_flags(pfs, fls);
      dmai_sc1<L_SB>(wk, (lane >> 3) * 4096 + (lane & 7) * 16, dws, O_ST2 + (64 * j + 8 * w) * 4096 + cl * 128);
      BAR();
    }
    if (s == 2) { VMWAIT(6); BAR(); }
    LS(22 + s);
  }
  {
    const f4 e0 = u ? o[3][0] : o[3][2], e1 = u ? o[3][1] : o[3][3], k0 = u ? o[3][2] : o[3][0], k1 = u ? o[3][3] : o[3][1];
    *(LAS f4*)(S + L_W1 + (ti * 7 + 6) * 4096 + (u ^ 1) * 2048 + l16) = e0;
    *(LAS f4*)(S + L_W1 + (ti * 7 + 6) * 4096 + (u ^ 1) * 2048 + 1024 + l16) = e1;
    f4 v0 = k0 + f4{pre[0], pre[1], pre[2], pre[3]}, v1 = k1 + f4{pre[4], pre[5], pre[6], pre[7]};
    f2 sv[4], xv[4];
#pragma unroll
    for (int k = 0; k < 4; ++k) { sv[k] = unpk(sp_[k]); xv[k] = unpk(xq[k]); asm volatile("" : "+v"(sv[k]), "+v"(xv[k])); }
    asm volatile("" : "+v"(v0), "+v"(v1));
    BAR();
#pragma unroll
    for (int k = 4; k < 7; ++k) { v0 += *(LAS f4*)(rown + k * 4096); v1 += *(LAS f4*)(rown + k * 4096 + 1024); }
    v4u r;
#pragma unroll
    for (int k = 0; k < 4; ++k) {
      const f2 v = k == 0 ? v0.xy : k == 1 ? v0.zw : k == 2 ? v1.xy : v1.zw;
      r[k] = pk2(fma2(sv[k], v * sigm2(v * sp2(-1.4426950409f)), xv[k]));
    }
    __builtin_amdgcn_raw_buffer_store_b128(r, __builtin_amdgcn_make_buffer_rsrc(out, 0, 1835008, 0x00020000), ((lz >> 4) & 15) * 896 + (lz >> 8) * 32, (cl * 4 + ti) * 14336 + 128 * j + 16 * u, AUX_SC1);
    STAMP(26);
    DUMP();
  }
}

extern "C" void kernel_launch(void* const* d_in, const int* in_sizes, int n_in, void* d_out, int out_size, void* d_ws, size_t ws_size,
                              hipStream_t stream) {
  fused_kernel<<<256, 512, 0, stream>>>((const u16*)d_in[0], (const u16*)d_in[1], (const u16*)d_in[2], (const u16*)d_in[3], (const u16*)d_in[4],
                                        (const u16*)d_in[5], (const u16*)d_in[6], (const u16*)d_in[7], (const u16*)d_in[8], (const u16*)d_in[10],
                                        (const u16*)d_in[11], (const u16*)d_in[12], (const u16*)d_in[13], (u16*)d_out, (unsigned char*)d_ws);
}
```
